```python
import torch

def _softmax(x, axis=-1):
    return torch.softmax(x, dim=axis)
def _gelu(x):
    return torch.nn.functional.gelu(x, approximate="tanh")
def _silu(x):
    return torch.nn.functional.silu(x)
def _sigmoid(x):
    return torch.sigmoid(x)
def _layernorm(x, axis=-1, eps=1e-5):
    m = x.mean(dim=axis, keepdim=True)
    v = x.var(dim=axis, keepdim=True, unbiased=False)
    return (x - m) / torch.sqrt(v + eps)
def _scatter_add(tgt, idx, src, axis=0):
    return tgt.clone().index_add_(axis, idx, src)
def _segment_sum(src, seg, n):
    out = torch.zeros((n,) + src.shape[1:], dtype=src.dtype)
    return out.index_add_(0, seg, src)
def _pad(x, pw):
    flat = tuple(v for pair in reversed(pw) for v in pair)
    return torch.nn.functional.pad(x, flat)

kernel_name = 'recurrent_scan__softmax_full_mha__dense_activation_squared_relu__prompt'

def setup_inputs():

    _w = (lambda a: (lambda t: t.to(torch.bfloat16) if t.is_floating_point() else t)(torch.as_tensor(a)))
    def _A_setup():
        B, S, D = 1, 2048, 512
        return {
            "x": _w((lambda *s: torch.randn(*s, dtype=torch.bfloat16) * 0.05)(B, S, D)),
            "wqkv": _w((lambda *s: torch.randn(*s, dtype=torch.bfloat16) * 0.05)(D, 3 * D)),
            "wo": _w((lambda *s: torch.randn(*s, dtype=torch.bfloat16) * 0.05)(D, D)),
        }
    _A_in = _A_setup()

    def _B_setup():
        B, S, D, F = 1, 2048, 512, 1792
        return {
            "x": _w((lambda *s: torch.randn(*s, dtype=torch.bfloat16) * 0.05)(B, S, D)),
            "w1": _w((lambda *s: torch.randn(*s, dtype=torch.bfloat16) * 0.05)(D, F)),
            "b1": _w((lambda *s: torch.randn(*s, dtype=torch.bfloat16) * 0.4)(F)),
            "w2": _w((lambda *s: torch.randn(*s, dtype=torch.bfloat16) * 0.05)(F, D)),
            "b2": _w((lambda *s: torch.randn(*s, dtype=torch.bfloat16) * 1.6)(D)),
        }
    _B_in = _B_setup()

    return {
        "A_x": _A_in["x"],
        "A_wqkv": _A_in["wqkv"],
        "A_wo": _A_in["wo"],
        "B_x": _B_in["x"],
        "B_w1": _B_in["w1"],
        "B_b1": _B_in["b1"],
        "B_w2": _B_in["w2"],
        "B_b2": _B_in["b2"],
    }

def reference(A_x, A_wqkv, A_wo, B_x, B_w1, B_b1, B_w2, B_b2):
    def _A_ref(x, wqkv, wo):
        B, S, D = x.shape
        H = 32; Dh = D // H
        qkv = (lambda x, *d: x.permute(*d))((x @ wqkv).reshape(B, S, 3, H, Dh), 2, 0, 3, 1, 4)
        q, k, v = qkv[0], qkv[1], qkv[2]
        scale = Dh ** -0.5
        scores = torch.einsum("bhqd,bhkd->bhqk", q, k) * scale
        p = _softmax(scores, axis=-1)
        o = torch.einsum("bhqk,bhkd->bhqd", p, v)
        return (lambda x, *d: x.permute(*d))(o, 0, 2, 1, 3).reshape(B, S, D) @ wo

    def _B_ref(x, w1, b1, w2, b2):
        h = torch.einsum("bsd,df->bsf", x, w1) + b1
        zero = torch.zeros_like(h)
        r = torch.where(h > zero, h, zero)
        h = r * r
        out = torch.einsum("bsf,fd->bsd", h, w2) + b2
        return out

    x = A_x
    xn = _layernorm(x, axis=-1)
    s = _A_ref(xn, A_wqkv, A_wo)
    g = _silu(_B_ref(xn, B_w1, B_b1, B_w2, B_b2))
    return x + s * g
```

```cpp
#include <hip/hip_runtime.h>
#include <hip/hip_bf16.h>
typedef __hip_bfloat16 bf16;
static __device__ float b2f(bf16 v) { return __bfloat162float(v); }
__global__ void ln_k(const bf16* x, float* xn) {
  int r = blockIdx.x; int t = threadIdx.x; __shared__ float s1[256], s2[256];
  float a = b2f(x[r*512+t]), b = b2f(x[r*512+256+t]);
  s1[t] = a + b; __syncthreads();
  for (int o = 128; o > 0; o >>= 1) { if (t < o) s1[t] += s1[t+o]; __syncthreads(); }
  float m = s1[0] / 512.f; s2[t] = (a-m)*(a-m) + (b-m)*(b-m); __syncthreads();
  for (int o = 128; o > 0; o >>= 1) { if (t < o) s2[t] += s2[t+o]; __syncthreads(); }
  float rs = rsqrtf(s2[0] / 512.f + 1e-5f);
  xn[r*512+t] = (a-m)*rs; xn[r*512+256+t] = (b-m)*rs;
}
__global__ void gemm_k(const float* A, const bf16* B, float* C, int N, int K, const bf16* bias, int mode) {
  __shared__ float As[16][17], Bs[16][17];
  int tx = threadIdx.x & 15, ty = threadIdx.x >> 4; int row = blockIdx.y*16+ty, col = blockIdx.x*16+tx; float acc = 0;
  for (int k0 = 0; k0 < K; k0 += 16) { As[ty][tx] = A[(size_t)row*K+k0+tx]; Bs[ty][tx] = b2f(B[(size_t)(k0+ty)*N+col]); __syncthreads();
    for (int k = 0; k < 16; ++k) acc += As[ty][k]*Bs[k][tx]; __syncthreads(); }
  if (bias) acc += b2f(bias[col]);
  if (mode == 1) { acc = acc > 0 ? acc : 0; acc *= acc; }
  if (mode == 2) { acc = acc / (1.f + __expf(-acc)); }
  C[(size_t)row*N+col] = acc;
}
__global__ void attn_k(const float* qkv, float* o) {
  int h = blockIdx.y, q = blockIdx.x*64 + threadIdx.x; float qv[16], acc[16];
  for (int d = 0; d < 16; ++d) { qv[d] = qkv[(size_t)q*1536 + h*16 + d]*0.25f; acc[d] = 0; }
  float m = -1e30f, l = 0;
  for (int k = 0; k < 2048; ++k) { const float* kp = qkv + (size_t)k*1536 + 512 + h*16; float s = 0; for (int d = 0; d < 16; ++d) s += qv[d]*kp[d];
    float mn = fmaxf(m, s); float a = __expf(m - mn), p = __expf(s - mn); l = l*a + p; for (int d = 0; d < 16; ++d) acc[d] = acc[d]*a + p*kp[512+d]; m = mn; }
  for (int d = 0; d < 16; ++d) o[(size_t)q*512 + h*16 + d] = acc[d]/l;
}
__global__ void fin_k(const bf16* x, const float* s, const float* g, bf16* out) { int i = blockIdx.x*256+threadIdx.x; out[i] = __float2bfloat16(b2f(x[i]) + s[i]*g[i]); }
extern "C" void kernel_launch(void* const* d_in, const int* in_sizes, int n_in, void* d_out, int out_size, void* d_ws, size_t ws_size, hipStream_t stream) {
  const bf16* x = (const bf16*)d_in[0]; const bf16* wqkv = (const bf16*)d_in[1]; const bf16* wo = (const bf16*)d_in[2];
  const bf16* w1 = (const bf16*)d_in[4]; const bf16* b1 = (const bf16*)d_in[5]; const bf16* w2 = (const bf16*)d_in[6]; const bf16* b2 = (const bf16*)d_in[7];
  float* ws = (float*)d_ws; float* xn = ws; float* qkv = xn + 2048*512; float* h = qkv + 2048*1536; float* o = h + 2048*1792; float* s = o + 2048*512; float* g = s + 2048*512;
  ln_k<<<2048, 256, 0, stream>>>(x, xn);
  gemm_k<<<dim3(1536/16, 128), 256, 0, stream>>>(xn, wqkv, qkv, 1536, 512, nullptr, 0);
  gemm_k<<<dim3(1792/16, 128), 256, 0, stream>>>(xn, w1, h, 1792, 512, b1, 1);
  gemm_k<<<dim3(512/16, 128), 256, 0, stream>>>(h, w2, g, 512, 1792, b2, 2);
  attn_k<<<dim3(32, 32), 64, 0, stream>>>(qkv, o);
  gemm_k<<<dim3(512/16, 128), 256, 0, stream>>>(o, wo, s, 512, 512, nullptr, 0);
  fin_k<<<2048*512/256, 256, 0, stream>>>(x, s, g, (bf16*)d_out);
}
```

```cpp
#include <hip/hip_runtime.h>
#include <hip/hip_bf16.h>

typedef __attribute__((ext_vector_type(8))) __bf16 bf16x8;
typedef __attribute__((ext_vector_type(16))) __bf16 bf16x16;
typedef __attribute__((ext_vector_type(2))) __bf16 bf16x2;
typedef __attribute__((ext_vector_type(16))) float f32x16;
typedef __attribute__((ext_vector_type(8))) float f32x8;
typedef __attribute__((ext_vector_type(4))) float f32x4;
typedef __attribute__((ext_vector_type(2))) float f32x2;
typedef __attribute__((ext_vector_type(4))) unsigned u32x4;
typedef __attribute__((ext_vector_type(8))) unsigned u32x8;
typedef __attribute__((ext_vector_type(2))) unsigned u32x2;
typedef __attribute__((ext_vector_type(4))) short s16x4;
#define LDS_AS __attribute__((address_space(3)))
#define DI __device__ __forceinline__

constexpr int S = 2048, D = 512, NH = 32, F = 1792;
constexpr size_t WS_Q = 0;
constexpr size_t WS_KV = WS_Q + (2u << 20);
constexpr size_t WS_H = WS_KV + (4u << 20);
constexpr size_t WS_W2 = WS_H + (7u << 20);
constexpr size_t WS_WO = WS_W2 + 1792u * 1024u;
constexpr size_t WS_O = WS_WO + 512u * 1024u;
constexpr size_t WS_G = WS_O + (2u << 20);
constexpr size_t WS_KMAX = WS_G + (2u << 20);

DI f32x16 mfma32(u32x4 a, u32x4 b, f32x16 c) {
  return __builtin_amdgcn_mfma_f32_32x32x16_bf16(__builtin_bit_cast(bf16x8, a), __builtin_bit_cast(bf16x8, b), c, 0, 0, 0);
}
DI unsigned pk_bf16(float lo, float hi) {
  f32x2 f = {lo, hi};
  return __builtin_bit_cast(unsigned, __builtin_convertvector(f, bf16x2));
}
DI float rbf(float x) { return (float)(__bf16)x; }
DI float dot2(unsigned a, unsigned b, float c) {
  return __builtin_amdgcn_fdot2_f32_bf16(__builtin_bit_cast(bf16x2, a), __builtin_bit_cast(bf16x2, b), c, false);
}
DI u32x4 lds_r128(unsigned addr) { return *(const LDS_AS u32x4*)(uintptr_t)addr; }
DI f32x4 lds_rf4(unsigned addr) { return *(const LDS_AS f32x4*)(uintptr_t)addr; }
DI float lds_rf(unsigned addr) { return *(const LDS_AS float*)(uintptr_t)addr; }
DI void lds_wf(unsigned addr, float v) { *(LDS_AS float*)(uintptr_t)addr = v; }
DI void lds_wf4(unsigned addr, f32x4 v) { *(LDS_AS f32x4*)(uintptr_t)addr = v; }
DI u32x2 lds_tr(unsigned addr) {
  return __builtin_bit_cast(u32x2, __builtin_amdgcn_ds_read_tr16_b64_v4i16((LDS_AS s16x4*)(uintptr_t)addr));
}
DI float swap_add(float v) {
  auto r = __builtin_amdgcn_permlane32_swap(__float_as_uint(v), __float_as_uint(v), false, false);
  return __uint_as_float(r[0]) + __uint_as_float(r[1]);
}
DI float swap_max(float v) {
  auto r = __builtin_amdgcn_permlane32_swap(__float_as_uint(v), __float_as_uint(v), false, false);
  return fmaxf(__uint_as_float(r[0]), __uint_as_float(r[1]));
}
DI void glds(const void* base, unsigned voff, unsigned lds) {
  asm volatile("s_mov_b32 m0, %2\n\ts_nop 0\n\tglobal_load_lds_dwordx4 %0, %1" ::"v"(voff), "s"(base), "s"(lds) : "memory");
}
#define BARRIER() asm volatile("s_waitcnt lgkmcnt(0)\n\ts_barrier" ::: "memory")
#define WAITV(n) asm volatile("s_waitcnt vmcnt(" #n ")" ::: "memory")

constexpr int K1_XB = 16384, K1_WB = 32768, K1_SLOT = K1_XB + K1_WB, K1_EX = 3 * K1_SLOT;
constexpr int K1_XS = K1_EX;
constexpr int K1_CS = K1_XS + 8192;
constexpr int K1_RS = K1_CS + 4096;
constexpr int K1_LDS = K1_RS + 2048;

template <bool VORI>
DI void k1_main(f32x16 (&acc)[2][2], float (&xs)[2], float (&xq)[2], float (&cs)[2], unsigned lds0, int lane, int wm, int wn) {
  const int r = lane & 31, hh = lane >> 5;
  const int rot = (wn + 2 * wm) & 3;
  const int q = (lane & 15) >> 2, p = lane & 3, ch = (lane >> 4) & 1;
  unsigned xa[4], wa[4];
  const int row = wm * 64 + r;
#pragma unroll
  for (int pos = 0; pos < 4; ++pos) {
    const int ks = (pos + rot) & 3;
    xa[pos] = lds0 + row * 128 + (((2 * ks + hh) ^ ((row >> 1) & 7)) << 4);
    wa[pos] = lds0 + K1_XB + (16 * ks + 8 * hh + q) * 512 + ((((2 * wn) ^ q) * 4 + ch * 2 + (p >> 1)) << 4) + (p & 1) * 8;
  }
  const unsigned ones = 0x3f803f80u;
  unsigned so = 0;
#pragma unroll 1
  for (int c = 0; c < 8; ++c) {
    BARRIER();
#pragma unroll
    for (int pos = 0; pos < 4; ++pos) {
      u32x4 xf[2], wf[2];
      xf[0] = lds_r128(xa[pos] + so);
      xf[1] = lds_r128(xa[pos] + so + 4096);
#pragma unroll
      for (int u = 0; u < 2; ++u) {
        const unsigned a = (wa[pos] ^ (u * 64)) + so;
        const u32x2 lo = lds_tr(a), hi = lds_tr(a + 2048);
        wf[u] = (u32x4){lo[0], lo[1], hi[0], hi[1]};
      }
#pragma unroll
      for (int um = 0; um < 2; ++um)
#pragma unroll
        for (int un = 0; un < 2; ++un)
          acc[um][un] = VORI ? mfma32(xf[um], wf[un], acc[um][un]) : mfma32(wf[un], xf[um], acc[um][un]);
      if (pos == 0) {
#pragma unroll
        for (int um = 0; um < 2; ++um)
#pragma unroll
          for (int i = 0; i < 4; ++i) { xs[um] = dot2(xf[um][i], ones, xs[um]); xq[um] = dot2(xf[um][i], xf[um][i], xq[um]); }
      }
      if (pos == 1 || pos == 2) {
#pragma unroll
        for (int un = 0; un < 2; ++un)
#pragma unroll
          for (int i = 0; i < 4; ++i) cs[un] = dot2(wf[un][i], ones, cs[un]);
      }
    }
    so = (so == 2 * K1_SLOT) ? 0 : so + K1_SLOT;
  }
}

__global__ __launch_bounds__(768, 3) void k1_kernel(const char* __restrict__ x, const char* __restrict__ wqkv, const char* __restrict__ w1,
                                                    const unsigned short* __restrict__ b1, const char* __restrict__ w2,
                                                    const char* __restrict__ wo, char* __restrict__ ws) {
  extern __shared__ __attribute__((aligned(16))) char smem[];
  const unsigned lds0 = (unsigned)(uintptr_t)(LDS_AS char*)smem;
  const int lane = threadIdx.x & 63, wid = __builtin_amdgcn_readfirstlane(threadIdx.x >> 6);
  const int xcd = blockIdx.x & 7, idx = blockIdx.x >> 3;
  if (idx >= 26) {
    const int f0 = (xcd * 6 + (idx - 26)) * 48 + wid * 4;
    const int band = f0 >> 4, nt0 = f0 & 15;
    const bool is2 = band < 112;
    const char* src = (is2 ? w2 + (size_t)band * 16384 : wo + (size_t)(band - 112) * 16384) + nt0 * 64;
    char* dst = ws + (is2 ? WS_W2 + (size_t)band * 1024 : WS_WO + (size_t)(band - 112) * 1024);
    const unsigned nstride = is2 ? 112u * 1024u : 32u * 1024u;
    const unsigned voff = (lane >> 2) * 1024 + (lane & 3) * 16;
    const unsigned lb = lds0 + wid * 4096;
#pragma unroll
    for (int i = 0; i < 4; ++i) glds(src + i * 64, voff, lb + i * 1024);
    WAITV(0);
    const int hh = lane >> 5, ch = (lane >> 4) & 1, q = (lane & 15) >> 2, p = lane & 3;
    const unsigned a = lb + (4 * hh + q) * 64 + ch * 32 + p * 8;
#pragma unroll
    for (int i = 0; i < 4; ++i) {
      const u32x2 lo = lds_tr(a + i * 1024), hi = lds_tr(a + i * 1024 + 512);
      *(u32x4*)(dst + (size_t)(nt0 + i) * nstride + lane * 16) = (u32x4){lo[0], lo[1], hi[0], hi[1]};
    }
    return;
  }
  const int mg = xcd & 1, ng = xcd >> 1;
  const int mt = idx < 24 ? mg * 8 + (idx & 7) : mg * 8 + ng * 2 + (idx - 24);
  const int nt = idx < 24 ? ng * 3 + (idx >> 3) : 12;
  if (wid >= 8) {
    const int w = wid - 8;
    const bool isq = nt < 6;
    const unsigned ldw = isq ? 3072u : 3584u;
    const char* wb = (isq ? wqkv + nt * 512 : w1 + (nt - 6) * 512) + (size_t)(16 * w) * ldw;
    const char* xb = x + (size_t)(mt * 128 + 32 * w) * 1024;
    unsigned vx[2], vw[2];
#pragma unroll
    for (int i = 0; i < 2; ++i) {
      vx[i] = (lane >> 3) * 1024 + (((lane & 7) ^ ((4 * i + (lane >> 4)) & 7)) << 4);
      vw[i] = (lane >> 5) * ldw + (((lane & 31) ^ ((2 * i + (lane >> 5)) << 2)) << 4);
    }
    const int start = (mt + 3 * nt) & 7;
    auto issue = [&](int c, unsigned so) {
      const int kc = (c + start) & 7;
      const char* xp = xb + kc * 128;
      const char* wp = wb + (size_t)kc * 64 * ldw;
#pragma unroll
      for (int i = 0; i < 4; ++i) glds(xp + i * 8192, vx[i & 1], lds0 + so + (32 * w + 8 * i) * 128);
#pragma unroll
      for (int i = 0; i < 8; ++i) glds(wp + (size_t)(2 * i) * ldw, vw[i & 1], lds0 + so + K1_XB + (16 * w + 2 * i) * 512);
    };
    issue(0, 0);
    issue(1, K1_SLOT);
    unsigned so = 2 * K1_SLOT;
#pragma unroll 1
    for (int c = 0; c < 6; ++c) {
      WAITV(12);
      BARRIER();
      issue(c + 2, so);
      so = (so == 2 * K1_SLOT) ? 0 : so + K1_SLOT;
    }
    WAITV(12);
    BARRIER();
    WAITV(0);
    BARRIER();
    return;
  }
  const int wm = wid >> 2, wn = wid & 3, r = lane & 31, hh = lane >> 5;
  const bool vori = (nt == 4 || nt == 5);
  float bias[2] = {0.f, 0.f};
  if (nt >= 6) {
#pragma unroll
    for (int un = 0; un < 2; ++un) bias[un] = __uint_as_float((unsigned)b1[(nt - 6) * 256 + wn * 64 + un * 32 + r] << 16);
  }
  f32x16 acc[2][2];
#pragma unroll
  for (int a = 0; a < 2; ++a)
#pragma unroll
    for (int b = 0; b < 2; ++b)
#pragma unroll
      for (int i = 0; i < 16; ++i) acc[a][b][i] = 0.f;
  float xs[2] = {0.f, 0.f}, xq[2] = {0.f, 0.f}, cs[2] = {0.f, 0.f};
  if (vori) k1_main<true>(acc, xs, xq, cs, lds0, lane, wm, wn);
  else k1_main<false>(acc, xs, xq, cs, lds0, lane, wm, wn);
#pragma unroll
  for (int u = 0; u < 2; ++u) {
    const unsigned t = lds0 + K1_XS + (wm * 64 + u * 32 + r) * 64 + (wn * 2 + hh) * 4;
    lds_wf(t, xs[u]);
    lds_wf(t + 32, xq[u]);
    lds_wf(lds0 + K1_CS + (wn * 64 + u * 32 + r) * 16 + (wm * 2 + hh) * 4, cs[u]);
  }
  BARRIER();
  float rstd[2];
  u32x4 xaug[2], waug[2];
#pragma unroll
  for (int u = 0; u < 2; ++u) {
    const unsigned t = lds0 + K1_XS + (wm * 64 + u * 32 + r) * 64;
    const f32x4 a0 = lds_rf4(t), a1 = lds_rf4(t + 16), q0 = lds_rf4(t + 32), q1 = lds_rf4(t + 48);
    const float sum = ((a0[0] + a0[1]) + (a0[2] + a0[3])) + ((a1[0] + a1[1]) + (a1[2] + a1[3]));
    const float sq = ((q0[0] + q0[1]) + (q0[2] + q0[3])) + ((q1[0] + q1[1]) + (q1[2] + q1[3]));
    const float mean = sum * (1.f / 512.f);
    const float var = fmaxf(sq * (1.f / 512.f) - mean * mean, 0.f);
    const float sd = rbf(sqrtf(rbf(rbf(var) + 1e-5f)));
    rstd[u] = 1.f / sd;
    xaug[u] = (u32x4){hh ? 0u : pk_bf16(-mean, -mean), hh ? 0u : pk_bf16(sd, 0.f), 0u, 0u};
    const f32x4 c4 = lds_rf4(lds0 + K1_CS + (wn * 64 + u * 32 + r) * 16);
    const float csum = (c4[0] + c4[1]) + (c4[2] + c4[3]);
    const float chi = rbf(csum);
    waug[u] = (u32x4){hh ? 0u : pk_bf16(chi, csum - chi), hh ? 0u : pk_bf16(bias[u], 0.f), 0u, 0u};
  }
#pragma unroll
  for (int um = 0; um < 2; ++um)
#pragma unroll
    for (int un = 0; un < 2; ++un)
      acc[um][un] = vori ? mfma32(xaug[um], waug[un], acc[um][un]) : mfma32(waug[un], xaug[um], acc[um][un]);
  const int tt0 = mt * 4 + wm * 2;
  if (nt >= 6) {
#pragma unroll
    for (int um = 0; um < 2; ++um)
#pragma unroll
      for (int un = 0; un < 2; ++un)
#pragma unroll
        for (int s = 0; s < 2; ++s) {
          u32x4 o;
#pragma unroll
          for (int i = 0; i < 4; ++i) {
            const float a = fmaxf(acc[um][un][8 * s + 2 * i] * rstd[um], 0.f), b = fmaxf(acc[um][un][8 * s + 2 * i + 1] * rstd[um], 0.f);
            o[i] = pk_bf16(a * a, b * b);
          }
          const int ks = (nt - 6) * 16 + wn * 4 + un * 2 + s;
          *(u32x4*)(ws + WS_H + ((size_t)(tt0 + um) * 112 + ks) * 1024 + lane * 16) = o;
        }
  } else if (nt < 4) {
    const bool isk = nt >= 2;
    const float sc = isk ? 1.f : 0.25f * 1.4426950408889634f;
    const int rp = (r & 19) | ((r & 4) << 1) | ((r & 8) >> 1);
#pragma unroll
    for (int um = 0; um < 2; ++um) {
      const float f = rstd[um] * sc;
#pragma unroll
      for (int un = 0; un < 2; ++un)
#pragma unroll
        for (int s = 0; s < 2; ++s) {
          u32x4 o;
#pragma unroll
          for (int i = 0; i < 4; ++i) o[i] = pk_bf16(acc[um][un][8 * s + 2 * i] * f, acc[um][un][8 * s + 2 * i + 1] * f);
          const int head = (nt & 1) * 16 + wn * 4 + un * 2 + s;
          if (!isk) {
            *(u32x4*)(ws + WS_Q + ((size_t)head * 64 + tt0 + um) * 1024 + lane * 16) = o;
          } else {
            *(u32x4*)(ws + WS_KV + ((size_t)head * 64 + tt0 + um) * 2048 + hh * 512 + rp * 16) = o;
            float n2 = dot2(o[0], o[0], dot2(o[1], o[1], dot2(o[2], o[2], dot2(o[3], o[3], 0.f))));
            n2 = swap_add(n2);
#pragma unroll
            for (int m = 16; m >= 1; m >>= 1) n2 = fmaxf(n2, __shfl_xor(n2, m));
            if (lane == 0) *(float*)(ws + WS_KMAX + ((size_t)head * 64 + tt0 + um) * 4) = n2;
          }
        }
    }
  } else {
    const unsigned rs = lds0 + K1_RS + wid * 256;
    lds_wf(rs + lane * 4, hh ? rstd[1] : rstd[0]);
#pragma unroll
    for (int um = 0; um < 2; ++um) {
      f32x4 rr[4];
#pragma unroll
      for (int g = 0; g < 4; ++g) rr[g] = lds_rf4(rs + (um * 32 + 8 * g + 4 * hh) * 4);
#pragma unroll
      for (int un = 0; un < 2; ++un)
#pragma unroll
        for (int s = 0; s < 2; ++s) {
          u32x4 o;
#pragma unroll
          for (int i = 0; i < 4; ++i) {
            const int e = 8 * s + 2 * i;
            o[i] = pk_bf16(acc[um][un][e] * rr[e >> 2][e & 3], acc[um][un][e + 1] * rr[e >> 2][(e & 3) + 1]);
          }
          const int head = (nt & 1) * 16 + wn * 4 + un * 2 + (r >> 4);
          *(u32x4*)(ws + WS_KV + ((size_t)head * 64 + tt0 + um) * 2048 + 1024 + hh * 512 + s * 256 + (r & 15) * 16) = o;
        }
    }
  }
}

constexpr int K2_KV = 0, K2_FX = 131072, K2_FLAG = K2_FX + 16384, K2_LDS = 163840;
constexpr float BOUND_LIMIT = 40.f;

#define E(r) "v_exp_f32 v" #r ", v" #r "\n\t"
#define QK(Dst, Q) "v_mfma_f32_32x32x16_bf16 v[" Dst "], v[120:123], v[" Q "], v[96:111]\n\t"
#define PV(O, V, P) "v_smfmac_f32_32x32x32_bf16 v[" O "], v[" V "], v[" P "], v144\n\t"
#define TILE(VP1, V0, V1, N0, N1, OK, OV0, OV1)                                                         \
  E(32) E(33) PV("80:95", VP1, "56:63") E(34) E(35) E(36) QK("48:63", "116:119")                        \
  "s_mov_b64 exec, %[msk]\n\t"                                                                          \
  "ds_read_b128 v[" N0 "], v145 offset:" OV0 "\n\t"                                                     \
  "ds_read_b128 v[" N1 "], v145 offset:" OV1 "\n\t"                                                     \
  "s_mov_b64 exec, -1\n\t"                                                                              \
  "ds_read_b128 v[120:123], v145 offset:" OK "\n\t"                                                     \
  E(37) E(38) E(39) E(40) E(41) E(42) PV("64:79", V0, "32:39") E(43) E(44) E(45) E(46) E(47)            \
  E(48) E(49) PV("64:79", V1, "40:47") E(50) E(51) E(52)                                                \
  "s_waitcnt lgkmcnt(0)\n\t"                                                                            \
  QK("32:47", "112:115")                                                                                \
  E(53) E(54) E(55) E(56) E(57) E(58) PV("80:95", V0, "48:55") E(59) E(60) E(61) E(62) E(63)

__global__ __launch_bounds__(768, 3) void k2_kernel(const unsigned short* __restrict__ b2, char* __restrict__ ws) {
  extern __shared__ __attribute__((aligned(16))) char smem[];
  const unsigned lds0 = (unsigned)(uintptr_t)(LDS_AS char*)smem;
  const int lane = threadIdx.x & 63, wid = __builtin_amdgcn_readfirstlane(threadIdx.x >> 6);
  const int xcd = blockIdx.x & 7, idx = blockIdx.x >> 3;
  const int head = xcd * 4 + (idx >> 3), qb = idx & 7;
  const char* kv = ws + WS_KV + (size_t)head * 131072;
  const int r = lane & 31, hh = lane >> 5;
  volatile LDS_AS unsigned* flags = (volatile LDS_AS unsigned*)(uintptr_t)(lds0 + K2_FLAG);
  if (wid >= 8) {
    const int w = wid - 8;
    if (w == 0 && lane < 16) flags[lane] = 0u;
    {
      const unsigned off = (w >> 1) * 65536 + 20480 + (w & 1) * 22528;
#pragma unroll
      for (int i = 0; i < 22; ++i) glds(kv + off + i * 1024, lane * 16, lds0 + K2_KV + off + i * 1024);
    }
    BARRIER();
    WAITV(0);
    if (lane == 0) __hip_atomic_fetch_add((LDS_AS unsigned*)&flags[0], 1u, __ATOMIC_RELAXED, __HIP_MEMORY_SCOPE_WORKGROUP);
    __builtin_amdgcn_s_setprio(2);
    const int mt = (xcd >> 1) * 8 + (idx & 7), nt = (xcd & 1) * 4 + (idx >> 3);
    const char* hp = ws + WS_H + ((size_t)(mt * 2) * 112 + w * 28) * 1024 + lane * 16;
    const char* wp = ws + WS_W2 + ((size_t)(nt * 2) * 112 + w * 28) * 1024 + lane * 16;
    f32x16 acc[2][2];
#pragma unroll
    for (int a = 0; a < 2; ++a)
#pragma unroll
      for (int b = 0; b < 2; ++b)
#pragma unroll
        for (int i = 0; i < 16; ++i) acc[a][b][i] = 0.f;
    constexpr int DEP = 4;
    u32x4 hf[DEP][2], wf[DEP][2];
#pragma unroll
    for (int s = 0; s < DEP; ++s)
#pragma unroll
      for (int u = 0; u < 2; ++u) {
        hf[s][u] = *(const u32x4*)(hp + (size_t)u * 112 * 1024 + s * 1024);
        wf[s][u] = *(const u32x4*)(wp + (size_t)u * 112 * 1024 + s * 1024);
      }
#pragma unroll
    for (int s = 0; s < 28; ++s) {
      const int b = s % DEP;
#pragma unroll
      for (int um = 0; um < 2; ++um)
#pragma unroll
        for (int un = 0; un < 2; ++un) acc[um][un] = mfma32(wf[b][un], hf[b][um], acc[um][un]);
      __builtin_amdgcn_sched_barrier(0);
      if (s + DEP < 28) {
#pragma unroll
        for (int u = 0; u < 2; ++u) {
          hf[b][u] = *(const u32x4*)(hp + (size_t)u * 112 * 1024 + (s + DEP) * 1024);
          wf[b][u] = *(const u32x4*)(wp + (size_t)u * 112 * 1024 + (s + DEP) * 1024);
        }
      }
      __builtin_amdgcn_sched_barrier(0);
    }
    f32x16 own = (w == 0) ? acc[0][0] : (w == 1) ? acc[0][1] : (w == 2) ? acc[1][0] : acc[1][1];
    unsigned wslot = w;
#pragma unroll
    for (int rd = 1; rd <= 3; ++rd) {
      const int t = (w + rd) & 3;
      const f32x16 snd = (t == 0) ? acc[0][0] : (t == 1) ? acc[0][1] : (t == 2) ? acc[1][0] : acc[1][1];
      const unsigned wa = lds0 + K2_FX + wslot * 4096 + lane * 16;
#pragma unroll
      for (int g = 0; g < 4; ++g) lds_wf4(wa + g * 1024, (f32x4){snd[4 * g], snd[4 * g + 1], snd[4 * g + 2], snd[4 * g + 3]});
      asm volatile("s_waitcnt lgkmcnt(0)" ::: "memory");
      if (lane == 0) flags[4 + w] = rd;
      const int src = (w - rd) & 3;
      while (__builtin_amdgcn_readfirstlane(flags[4 + src]) < (unsigned)rd) __builtin_amdgcn_s_sleep(1);
      asm volatile("" ::: "memory");
      const unsigned rslot = (w - (rd * (rd + 1)) / 2) & 3;
      const unsigned ra = lds0 + K2_FX + rslot * 4096 + lane * 16;
#pragma unroll
      for (int g = 0; g < 4; ++g) {
        const f32x4 v = lds_rf4(ra + g * 1024);
#pragma unroll
        for (int i = 0; i < 4; ++i) own[4 * g + i] += v[i];
      }
      asm volatile("s_waitcnt lgkmcnt(0)" ::: "memory");
      wslot = rslot;
    }
    const int tok = mt * 64 + (w >> 1) * 32 + r, n0 = nt * 64 + (w & 1) * 32 + 4 * hh;
#pragma unroll
    for (int g = 0; g < 4; ++g) {
      const u32x2 bb = *(const u32x2*)(b2 + n0 + 8 * g);
      float y[4];
#pragma unroll
      for (int i = 0; i < 4; ++i) {
        const float z = own[4 * g + i] + __uint_as_float((i & 1) ? (bb[i >> 1] & 0xffff0000u) : (bb[i >> 1] << 16));
        y[i] = z * __builtin_amdgcn_rcpf(1.f + __builtin_amdgcn_exp2f(z * -1.4426950408889634f));
      }
      *(u32x2*)(ws + WS_G + ((size_t)tok * 512 + n0 + 8 * g) * 2) = (u32x2){pk_bf16(y[0], y[1]), pk_bf16(y[2], y[3])};
    }
    return;
  }
  const int qg = wid & 3, kh = wid >> 2;
  const int qt0 = qb * 8 + qg * 2;
  u32x4 qa, qbf;
  float km;
  {
    const char* qp = ws + WS_Q + ((size_t)head * 64 + qt0) * 1024;
    const char* kp = ws + WS_KMAX + (size_t)head * 256;
    asm volatile("global_load_dwordx4 %0, %3, %4\n\tglobal_load_dwordx4 %1, %3, %4 offset:1024\n\tglobal_load_dword %2, %5, %6"
                 : "=&v"(qa), "=&v"(qbf), "=&v"(km)
                 : "v"(lane * 16), "s"(qp), "v"(lane * 4), "s"(kp)
                 : "memory");
    const unsigned off = kh * 65536 + qg * 5120;
#pragma unroll
    for (int i = 0; i < 5; ++i) glds(kv + off + i * 1024, lane * 16, lds0 + K2_KV + off + i * 1024);
    asm volatile("s_waitcnt vmcnt(5)" : "+v"(qa), "+v"(qbf), "+v"(km)::"memory");
  }
#pragma unroll
  for (int m = 32; m >= 1; m >>= 1) km = fmaxf(km, __shfl_xor(km, m));
  float na = swap_add(dot2(qa[0], qa[0], dot2(qa[1], qa[1], dot2(qa[2], qa[2], dot2(qa[3], qa[3], 0.f)))));
  float nb = swap_add(dot2(qbf[0], qbf[0], dot2(qbf[1], qbf[1], dot2(qbf[2], qbf[2], dot2(qbf[3], qbf[3], 0.f)))));
  const float bound = sqrtf(fmaxf(na, nb) * km) * 1.001f;
  const bool fast = __all(bound <= BOUND_LIMIT);
  WAITV(0);
  BARRIER();
  f32x16 oa, ob;
  float ma, mb;
  const unsigned kvb = lds0 + K2_KV + kh * 65536 + lane * 16;
  if (fast) {
    f32x16 cneg;
#pragma unroll
    for (int i = 0; i < 16; ++i) { cneg[i] = -bound; oa[i] = 0.f; ob[i] = 0.f; }
    unsigned addr = kvb;
    const unsigned fl = lds0 + K2_FLAG;
    asm volatile(
        "v_mov_b32 v144, 0xdddd\n\t"
        "v_mov_b32 v128, 0x3f803f80\n\tv_mov_b32 v129, v128\n\tv_mov_b32 v130, v128\n\tv_mov_b32 v131, v128\n\t"
        "v_mov_b32 v132, v128\n\tv_mov_b32 v133, v128\n\tv_mov_b32 v134, v128\n\tv_mov_b32 v135, v128\n\t"
        "v_mov_b32 v136, v128\n\tv_mov_b32 v137, v128\n\tv_mov_b32 v138, v128\n\tv_mov_b32 v139, v128\n\t"
        "v_mov_b32 v140, v128\n\tv_mov_b32 v141, v128\n\tv_mov_b32 v142, v128\n\tv_mov_b32 v143, v128\n\t"
        "ds_read_b128 v[120:123], v145\n\t"
        "s_mov_b64 exec, %[msk]\n\t"
        "ds_read_b128 v[128:131], v145 offset:1024\n\t"
        "ds_read_b128 v[132:135], v145 offset:1280\n\t"
        "s_mov_b64 exec, -1\n\t"
        "v_mov_b32 v56, 0\n\tv_mov_b32 v57, 0\n\tv_mov_b32 v58, 0\n\tv_mov_b32 v59, 0\n\t"
        "v_mov_b32 v60, 0\n\tv_mov_b32 v61, 0\n\tv_mov_b32 v62, 0\n\tv_mov_b32 v63, 0\n\t"
        "s_waitcnt lgkmcnt(0)\n\t"
        QK("32:47", "112:115")
        "s_mov_b32 s40, 0\n\t"
        "s_nop 10\n\t"
        "1:\n\t"
        "s_cmp_lg_u32 s40, 4\n\t"
        "s_cbranch_scc1 3f\n\t"
        "2:\n\t"
        "ds_read_b32 v124, %[fl]\n\t"
        "s_waitcnt lgkmcnt(0)\n\t"
        "v_readfirstlane_b32 s41, v124\n\t"
        "s_cmp_lt_u32 s41, 4\n\t"
        "s_cbranch_scc1 2b\n\t"
        "3:\n\t"
        TILE("140:143", "128:131", "132:135", "136:139", "140:143", "2048", "3072", "3328")
        TILE("132:135", "136:139", "140:143", "128:131", "132:135", "4096", "5120", "5376")
        "v_add_u32 v145, 0x1000, v145\n\t"
        "s_add_u32 s40, s40, 1\n\t"
        "s_cmp_lt_u32 s40, 16\n\t"
        "s_cbranch_scc1 1b\n\t"
        PV("80:95", "140:143", "56:63")
        "s_nop 15\n\t"
        : "+{v[64:79]}"(oa), "+{v[80:95]}"(ob), "+{v145}"(addr)
        : "{v[96:111]}"(cneg), "{v[112:115]}"(qa), "{v[116:119]}"(qbf), [msk] "s"(0x0000FFFF0000FFFFull), [fl] "v"(fl)
        : "memory", "scc", "s40", "s41", "v32", "v33", "v34", "v35", "v36", "v37", "v38", "v39", "v40", "v41", "v42", "v43", "v44", "v45", "v46", "v47",
          "v48", "v49", "v50", "v51", "v52", "v53", "v54", "v55", "v56", "v57", "v58", "v59", "v60", "v61", "v62", "v63", "v120", "v121", "v122",
          "v123", "v124", "v128", "v129", "v130", "v131", "v132", "v133", "v134", "v135", "v136", "v137", "v138", "v139", "v140", "v141", "v142",
          "v143", "v144");
    ma = bound; mb = bound;
  } else {
    while (__builtin_amdgcn_readfirstlane(flags[0]) < 4u) __builtin_amdgcn_s_sleep(1);
    asm volatile("" ::: "memory");
    f32x16 z;
#pragma unroll
    for (int i = 0; i < 16; ++i) { z[i] = 0.f; oa[i] = 0.f; ob[i] = 0.f; }
    ma = -3.0e38f; mb = -3.0e38f;
#pragma unroll 1
    for (int t = 0; t < 32; ++t) {
      const u32x4 kf = lds_r128(kvb + t * 2048);
      u32x4 v0 = lds_r128(kvb + t * 2048 + 1024 - (r & 16) * 16), v1 = lds_r128(kvb + t * 2048 + 1280 - (r & 16) * 16);
      if (r >= 16) { v0 = (u32x4){0x3f803f80u, 0x3f803f80u, 0x3f803f80u, 0x3f803f80u}; v1 = v0; }
#pragma unroll
      for (int u = 0; u < 2; ++u) {
        f32x16 s = mfma32(kf, u ? qbf : qa, z);
        float mx = s[0];
#pragma unroll
        for (int i = 1; i < 16; ++i) mx = fmaxf(mx, s[i]);
        mx = swap_max(mx);
        const float mo = u ? mb : ma, mn = fmaxf(mo, mx), al = __builtin_amdgcn_exp2f(mo - mn);
        f32x16 o = u ? ob : oa;
#pragma unroll
        for (int i = 0; i < 16; ++i) { o[i] *= al; s[i] = __builtin_amdgcn_exp2f(s[i] - mn); }
        const f32x8 plo = __builtin_shufflevector(s, s, 0, 1, 2, 3, 4, 5, 6, 7), phi = __builtin_shufflevector(s, s, 8, 9, 10, 11, 12, 13, 14, 15);
        o = __builtin_amdgcn_smfmac_f32_32x32x32_bf16(__builtin_bit_cast(bf16x8, v0), __builtin_bit_cast(bf16x16, plo), o, 0xdddd, 0, 0);
        o = __builtin_amdgcn_smfmac_f32_32x32x32_bf16(__builtin_bit_cast(bf16x8, v1), __builtin_bit_cast(bf16x16, phi), o, 0xdddd, 0, 0);
        if (u) { ob = o; mb = mn; } else { oa = o; ma = mn; }
      }
    }
  }
  BARRIER();
  {
    const f32x16 snd = kh ? oa : ob;
    const unsigned wa = lds0 + wid * 4096 + lane * 16;
    lds_wf4(wa, (f32x4){snd[0], snd[1], snd[2], snd[3]});
    lds_wf4(wa + 1024, (f32x4){snd[4], snd[5], snd[6], snd[7]});
    lds_wf4(wa + 2048, (f32x4){snd[8], kh ? ma : mb, 0.f, 0.f});
  }
  BARRIER();
  {
    const f32x16 own = kh ? ob : oa;
    const float mo = kh ? mb : ma;
    const unsigned ra = lds0 + (wid ^ 4) * 4096 + lane * 16;
    const f32x4 a = lds_rf4(ra), b = lds_rf4(ra + 1024), c = lds_rf4(ra + 2048);
    const float mn = fmaxf(mo, c[1]);
    const float f0 = __builtin_amdgcn_exp2f(mo - mn), f1 = __builtin_amdgcn_exp2f(c[1] - mn);
    const float inv = 1.f / (own[8] * f0 + c[0] * f1);
    const float g0 = f0 * inv, g1 = f1 * inv;
    u32x4 o;
    o[0] = pk_bf16(own[0] * g0 + a[0] * g1, own[1] * g0 + a[1] * g1);
    o[1] = pk_bf16(own[2] * g0 + a[2] * g1, own[3] * g0 + a[3] * g1);
    o[2] = pk_bf16(own[4] * g0 + b[0] * g1, own[5] * g0 + b[1] * g1);
    o[3] = pk_bf16(own[6] * g0 + b[2] * g1, own[7] * g0 + b[3] * g1);
    *(u32x4*)(ws + WS_O + ((size_t)(qt0 + kh) * 32 + head) * 1024 + lane * 16) = o;
  }
}

constexpr int K3_LDS = 4 * 64 * 68 * 4;
__global__ __launch_bounds__(256) void k3_kernel(const char* __restrict__ x, const char* __restrict__ ws, char* __restrict__ out) {
  extern __shared__ __attribute__((aligned(16))) char smem[];
  const unsigned lds0 = (unsigned)(uintptr_t)(LDS_AS char*)smem;
  const int lane = threadIdx.x & 63, w = __builtin_amdgcn_readfirstlane(threadIdx.x >> 6);
  const int xcd = blockIdx.x & 7, idx = blockIdx.x >> 3;
  const int mt = (xcd >> 1) * 8 + (idx & 7), nt = (xcd & 1) * 4 + (idx >> 3);
  const char* op = ws + WS_O + ((size_t)(mt * 2) * 32 + w * 8) * 1024 + lane * 16;
  const char* wp = ws + WS_WO + ((size_t)(nt * 2) * 32 + w * 8) * 1024 + lane * 16;
  u32x4 of[8][2], wf[8][2];
#pragma unroll
  for (int s = 0; s < 8; ++s)
#pragma unroll
    for (int u = 0; u < 2; ++u) {
      of[s][u] = *(const u32x4*)(op + u * 32768 + s * 1024);
      wf[s][u] = *(const u32x4*)(wp + u * 32768 + s * 1024);
    }
  const int row = threadIdx.x >> 2, c0 = (threadIdx.x & 3) * 16;
  const size_t eo = ((size_t)(mt * 64 + row) * 512 + nt * 64 + c0) * 2;
  const u32x4 xv0 = *(const u32x4*)(x + eo), xv1 = *(const u32x4*)(x + eo + 16);
  const u32x4 gv0 = *(const u32x4*)(ws + WS_G + eo), gv1 = *(const u32x4*)(ws + WS_G + eo + 16);
  __builtin_amdgcn_sched_barrier(0);
  f32x16 acc[2][2];
#pragma unroll
  for (int a = 0; a < 2; ++a)
#pragma unroll
    for (int b = 0; b < 2; ++b)
#pragma unroll
      for (int i = 0; i < 16; ++i) acc[a][b][i] = 0.f;
#pragma unroll
  for (int s = 0; s < 8; ++s)
#pragma unroll
    for (int um = 0; um < 2; ++um)
#pragma unroll
      for (int un = 0; un < 2; ++un) acc[um][un] = mfma32(wf[s][un], of[s][um], acc[um][un]);
  const int r = lane & 31, hh = lane >> 5;
#pragma unroll
  for (int um = 0; um < 2; ++um)
#pragma unroll
    for (int un = 0; un < 2; ++un)
#pragma unroll
      for (int g = 0; g < 4; ++g)
        lds_wf4(lds0 + ((w * 64 + um * 32 + r) * 68 + un * 32 + 8 * g + 4 * hh) * 4,
                (f32x4){acc[um][un][4 * g], acc[um][un][4 * g + 1], acc[um][un][4 * g + 2], acc[um][un][4 * g + 3]});
  __syncthreads();
  u32x4 res[2];
#pragma unroll
  for (int h2 = 0; h2 < 2; ++h2) {
    const u32x4 xv = h2 ? xv1 : xv0, gv = h2 ? gv1 : gv0;
#pragma unroll
    for (int k = 0; k < 2; ++k) {
      f32x4 s4 = lds_rf4(lds0 + (row * 68 + c0 + h2 * 8 + k * 4) * 4);
#pragma unroll
      for (int p = 1; p < 4; ++p) s4 += lds_rf4(lds0 + ((p * 64 + row) * 68 + c0 + h2 * 8 + k * 4) * 4);
#pragma unroll
      for (int i = 0; i < 2; ++i) {
        const unsigned xw = xv[2 * k + i], gw = gv[2 * k + i];
        res[h2][2 * k + i] = pk_bf16(__uint_as_float(xw << 16) + s4[2 * i] * __uint_as_float(gw << 16),
                                     __uint_as_float(xw & 0xffff0000u) + s4[2 * i + 1] * __uint_as_float(gw & 0xffff0000u));
      }
    }
  }
  *(u32x4*)(out + eo) = res[0];
  *(u32x4*)(out + eo + 16) = res[1];
}

extern "C" void kernel_launch(void* const* d_in, const int* in_sizes, int n_in, void* d_out, int out_size, void* d_ws, size_t ws_size,
                              hipStream_t stream) {
  const char* x = (const char*)d_in[0];
  const char* wqkv = (const char*)d_in[1];
  const char* wo = (const char*)d_in[2];
  const char* w1 = (const char*)d_in[4];
  const unsigned short* b1 = (const unsigned short*)d_in[5];
  const char* w2 = (const char*)d_in[6];
  const unsigned short* b2 = (const unsigned short*)d_in[7];
  char* ws = (char*)d_ws;
  (void)hipFuncSetAttribute((const void*)k1_kernel, hipFuncAttributeMaxDynamicSharedMemorySize, K1_LDS);
  (void)hipFuncSetAttribute((const void*)k2_kernel, hipFuncAttributeMaxDynamicSharedMemorySize, K2_LDS);
  (void)hipFuncSetAttribute((const void*)k3_kernel, hipFuncAttributeMaxDynamicSharedMemorySize, K3_LDS);
  k1_kernel<<<256, 768, K1_LDS, stream>>>(x, wqkv, w1, b1, w2, wo, ws);
  k2_kernel<<<256, 768, K2_LDS, stream>>>(b2, ws);
  k3_kernel<<<256, 256, K3_LDS, stream>>>(x, ws, (char*)d_out);
}
```

```cpp
#include <hip/hip_runtime.h>
#include <hip/hip_bf16.h>

typedef __attribute__((ext_vector_type(8))) __bf16 bf16x8;
typedef __attribute__((ext_vector_type(16))) __bf16 bf16x16;
typedef __attribute__((ext_vector_type(2))) __bf16 bf16x2;
typedef __attribute__((ext_vector_type(16))) float f32x16;
typedef __attribute__((ext_vector_type(8))) float f32x8;
typedef __attribute__((ext_vector_type(4))) float f32x4;
typedef __attribute__((ext_vector_type(2))) float f32x2;
typedef __attribute__((ext_vector_type(4))) unsigned u32x4;
typedef __attribute__((ext_vector_type(8))) unsigned u32x8;
typedef __attribute__((ext_vector_type(32))) unsigned u32x32;
typedef __attribute__((ext_vector_type(2))) unsigned u32x2;
typedef __attribute__((ext_vector_type(4))) short s16x4;
typedef unsigned long long u64;
#define LDS_AS __attribute__((address_space(3)))
#define GLB_AS __attribute__((address_space(1)))
#define DI __device__ __forceinline__

constexpr unsigned WS_FLAG = 0;
constexpr unsigned WS_FA = 1024;
constexpr unsigned WS_FB = 4096;
constexpr unsigned WS_FC = 8192;
constexpr unsigned WS_KMAX = 16384;
constexpr unsigned WS_Q = 32768;
constexpr unsigned WS_KV = WS_Q + (2u << 20);
constexpr unsigned WS_H = WS_KV + (4u << 20);
constexpr unsigned WS_W2 = WS_H + (7u << 20);
constexpr unsigned WS_WO = WS_W2 + 1792u * 1024u;
constexpr unsigned WS_O = WS_WO + 512u * 1024u;
constexpr unsigned WS_END = WS_O + (2u << 20);

constexpr unsigned U = 16384;
constexpr unsigned L_MISC = 9 * U, L_AUG = L_MISC, L_STAT = L_MISC + 4096, L_CNT = L_MISC + 6144, L_CONST = L_CNT + 128,
                   L_QF = L_MISC + 7168, L_KM = L_QF + 8192, L_TOTAL = 10 * U;
enum { C_DRAIN = 0, C_CS, C_WDONE, C_KV1, C_KV2, C_HDRAIN, C_PROG, C_ADONE, C_WO, C_FX, C_G, C_AX, C_PAIR   };
constexpr float BOUND_LIMIT = 40.f;
#define NBG 8

#define TS(i)
#define TSFLUSH()

DI f32x16 mfma32(u32x4 a, u32x4 b, f32x16 c) {
  return __builtin_amdgcn_mfma_f32_32x32x16_bf16(__builtin_bit_cast(bf16x8, a), __builtin_bit_cast(bf16x8, b), c, 0, 0, 0);
}
DI f32x4 fragsum(u32x4 sel, int selidx, u32x4 f0, u32x4 f1, f32x4 c) {
  const u32x8 b = __builtin_shufflevector(f0, f1, 0, 1, 2, 3, 4, 5, 6, 7);
  return __builtin_amdgcn_smfmac_f32_16x16x64_bf16(__builtin_bit_cast(bf16x8, sel), __builtin_bit_cast(bf16x16, b), c, selidx, 0, 0);
}
DI unsigned pk_bf16(float lo, float hi) {
  f32x2 f = {lo, hi};
  return __builtin_bit_cast(unsigned, __builtin_convertvector(f, bf16x2));
}
DI float rbf(float x) { return (float)(__bf16)x; }
DI float dot2(unsigned a, unsigned b, float c) {
  return __builtin_amdgcn_fdot2_f32_bf16(__builtin_bit_cast(bf16x2, a), __builtin_bit_cast(bf16x2, b), c, false);
}
DI float sq8(u32x4 v) { return dot2(v[0], v[0], dot2(v[1], v[1], dot2(v[2], v[2], dot2(v[3], v[3], 0.f)))); }
DI u32x4 lds_r128(unsigned addr) { return *(const LDS_AS u32x4*)(uintptr_t)addr; }
DI f32x4 lds_rf4(unsigned addr) { return *(const LDS_AS f32x4*)(uintptr_t)addr; }
DI float lds_rf(unsigned addr) { return *(const LDS_AS float*)(uintptr_t)addr; }
DI void lds_wu(unsigned addr, unsigned v) { *(LDS_AS unsigned*)(uintptr_t)addr = v; }
DI void lds_wf(unsigned addr, float v) { *(LDS_AS float*)(uintptr_t)addr = v; }
DI void lds_wf2(unsigned addr, f32x2 v) { *(LDS_AS f32x2*)(uintptr_t)addr = v; }
DI void lds_wf4(unsigned addr, f32x4 v) { *(LDS_AS f32x4*)(uintptr_t)addr = v; }
DI void lds_w128(unsigned addr, u32x4 v) { *(LDS_AS u32x4*)(uintptr_t)addr = v; }
DI u32x2 lds_tr(unsigned addr) {
  return __builtin_bit_cast(u32x2, __builtin_amdgcn_ds_read_tr16_b64_v4i16((LDS_AS s16x4*)(uintptr_t)addr));
}
DI u32x4 lds_trf(unsigned addr, unsigned step) {
  const u32x2 lo = lds_tr(addr), hi = lds_tr(addr + step);
  return (u32x4){lo[0], lo[1], hi[0], hi[1]};
}
DI float swap_add(float v) {
  auto r = __builtin_amdgcn_permlane32_swap(__float_as_uint(v), __float_as_uint(v), false, false);
  return __uint_as_float(r[0]) + __uint_as_float(r[1]);
}
DI float swap_max(float v) {
  auto r = __builtin_amdgcn_permlane32_swap(__float_as_uint(v), __float_as_uint(v), false, false);
  return fmaxf(__uint_as_float(r[0]), __uint_as_float(r[1]));
}
DI unsigned umax(unsigned a, unsigned b) { return a > b ? a : b; }
DI unsigned umax32(unsigned m) {
  m = umax(m, (unsigned)__builtin_amdgcn_update_dpp(0, (int)m, 0xB1, 0xf, 0xf, true));
  m = umax(m, (unsigned)__builtin_amdgcn_update_dpp(0, (int)m, 0x4E, 0xf, 0xf, true));
  m = umax(m, (unsigned)__builtin_amdgcn_update_dpp(0, (int)m, 0x141, 0xf, 0xf, true));
  m = umax(m, (unsigned)__builtin_amdgcn_update_dpp(0, (int)m, 0x140, 0xf, 0xf, true));
  auto r = __builtin_amdgcn_permlane16_swap(m, m, false, false);
  return umax(r[0], r[1]);
}
DI void glds(const void* base, unsigned voff, unsigned lds) {
  asm volatile("s_mov_b32 m0, %2\n\ts_nop 0\n\tglobal_load_lds_dwordx4 %0, %1" ::"v"(voff), "s"(base), "s"(lds) : "memory");
}
DI void glds4_dev(const void* base, unsigned voff, unsigned lds) {
  asm volatile("s_mov_b32 m0, %2\n\ts_nop 0\n\tglobal_load_lds_dwordx4 %0, %1 sc1\n\tglobal_load_lds_dwordx4 %0, %1 offset:1024 sc1\n\t"
               "global_load_lds_dwordx4 %0, %1 offset:2048 sc1\n\tglobal_load_lds_dwordx4 %0, %1 offset:3072 sc1" ::"v"(voff), "s"(base), "s"(lds) : "memory");
}
DI void glds2_dev(const void* base, unsigned voff, unsigned lds) {
  asm volatile("s_mov_b32 m0, %2\n\ts_nop 0\n\tglobal_load_lds_dwordx4 %0, %1 sc1\n\tglobal_load_lds_dwordx4 %0, %1 offset:1024 sc1" ::"v"(voff), "s"(base), "s"(lds) : "memory");
}
DI void st16_dev(void* base, unsigned voff, u32x4 v) {
  asm volatile("global_store_dwordx4 %0, %1, %2 sc1\n\ts_nop 1" ::"v"(voff), "v"(v), "s"(base) : "memory");
}
DI void st4_dev2(void* base, unsigned voff, unsigned v) {
  asm volatile("s_mov_b64 exec, %3\n\tglobal_store_dword %0, %1, %2 sc1\n\ts_mov_b64 exec, -1" ::"v"(voff), "v"(v), "s"(base), "s"(0x100000001ull) : "memory");
}
DI void st8_dev1(void* base, unsigned voff, u64 v) {
  asm volatile("s_mov_b64 exec, 1\n\tglobal_store_dwordx2 %0, %1, %2 sc1\n\ts_mov_b64 exec, -1" ::"v"(voff), "v"(v), "s"(base) : "memory");
}
DI unsigned pin(unsigned v) { asm volatile("" : "+v"(v)); return v; }
#define BARRIER() asm volatile("s_waitcnt lgkmcnt(0)\n\ts_barrier" ::: "memory")
#define WAITV(n) asm volatile("s_waitcnt vmcnt(" #n ")" ::: "memory")
#define WAITL() asm volatile("s_waitcnt lgkmcnt(0)" ::: "memory")
#define SB() __builtin_amdgcn_sched_barrier(0)
template <int C> DI void cnt_add() {
  asm volatile("s_waitcnt lgkmcnt(0)\n\ts_mov_b64 exec, 1\n\tds_add_u32 %0, %1 offset:%c2\n\ts_mov_b64 exec, -1" ::"v"(L_CNT), "v"(1u), "i"(4 * C) : "memory");
}
template <int C, int N, int SLEEP = 1> DI void cnt_wait() {
  unsigned t, i, v;
  asm volatile("s_mov_b32 %1, 0\n\ts_branch 2f\n\t1:\n\ts_sleep %c6\n\t2:\n\tds_read_b32 %0, %3 offset:%c4\n\ts_add_u32 %1, %1, 1\n\ts_waitcnt lgkmcnt(0)\n\t"
               "v_readfirstlane_b32 %2, %0\n\ts_bitcmp1_b32 %1, 20\n\ts_cselect_b32 %2, %c5, %2\n\ts_cmp_lt_u32 %2, %c5\n\ts_cbranch_scc1 1b"
               : "=&v"(t), "=&s"(i), "=&s"(v) : "v"(L_CNT), "i"(4 * C), "i"(N), "i"(SLEEP) : "memory", "scc");
}
template <int SLEEP = 2> DI void poll_flags(const char* ws, unsigned voff, u64 tag) {
  u64 v; unsigned i;
  asm volatile("s_mov_b32 %1, 0\n\ts_branch 2f\n\t1:\n\ts_sleep %c5\n\t2:\n\tglobal_load_dwordx2 %0, %2, %3 sc1\n\ts_add_u32 %1, %1, 1\n\ts_waitcnt vmcnt(0)\n\t"
               "v_cmp_ne_u64 vcc, %4, %0\n\ts_bitcmp1_b32 %1, 17\n\ts_cbranch_scc1 3f\n\ts_cbranch_vccnz 1b\n\t3:"
               : "=&v"(v), "=&s"(i) : "v"(voff), "s"(ws), "s"(tag), "i"(SLEEP) : "memory", "scc", "vcc");
}

#define CLOB_W "v64", "v65", "v66", "v67", "v68", "v69", "v70", "v71", "v72", "v73", "v74", "v75", "v76", "v77", "v78", "v79", "v80", "v81", "v82", \
               "v83", "v84", "v85", "v86", "v87", "v88", "v89", "v90", "v91", "v92", "v93", "v94", "v95"
#define ST_RD(I, XA, WA, WR) \
  "ds_read_b128 v[xb+4*" #I ":xb+4*" #I "+3], %[" XA "] offset:xo\n\t" \
  "ds_read_b64_tr_b16 v[64+4*" #I ":65+4*" #I "], %[" WA "] offset:wo+1024*" #WR "\n\t" \
  "ds_read_b64_tr_b16 v[66+4*" #I ":67+4*" #I "], %[" WA "] offset:wo+1024*" #WR "+256\n\t"
#define ST_MM(I, N) "s_waitcnt lgkmcnt(" #N ")\n\tv_mfma_f32_32x32x16_bf16 v[96:111], v[64+4*" #I ":67+4*" #I "], v[xb+4*" #I ":xb+4*" #I "+3], v[96:111]\n\t"
#define STAGE_TEXT \
  ".set xb, 128+32*%c[s]\n\t.set xo, (%c[s]&1)*32768\n\t.set wo, (%c[s]/2)*32768+(%c[s]&1)*8192\n\t" \
  ST_RD(0, "a0", "wa", 0) ST_RD(1, "a1", "wa", 1) ST_RD(2, "a2", "wa", 2) ST_RD(3, "a3", "wa", 3) \
  ".if %c[s]==0\n\ts_waitcnt lgkmcnt(9)\n\tv_mfma_f32_32x32x16_bf16 v[96:111], v[64:67], v[xb:xb+3], 0\n\t.else\n\t" ST_MM(0, 9) ".endif\n\t" \
  ST_RD(4, "a4", "wb", 0) ST_MM(1, 9) ST_RD(5, "a5", "wb", 1) ST_MM(2, 9) ST_RD(6, "a6", "wb", 2) ST_MM(3, 9) ST_RD(7, "a7", "wb", 3) ST_MM(4, 9) \
  "ds_read_b64_tr_b16 v[64:65], %[wc] offset:wo\n\tds_read_b64_tr_b16 v[66:67], %[wc] offset:wo+256\n\t" \
  "ds_read_b64_tr_b16 v[68:69], %[wc] offset:wo+1024\n\tds_read_b64_tr_b16 v[70:71], %[wc] offset:wo+1280\n\t" \
  ST_MM(5, 10) ST_MM(6, 7) ST_MM(7, 4) \
  "v_smfmac_f32_16x16x64_bf16 %[xs], %[sel], v[xb:xb+7], %[si]\n\tv_smfmac_f32_16x16x64_bf16 %[xs], %[sel], v[xb+8:xb+15], %[si]\n\t" \
  "s_waitcnt lgkmcnt(0)\n\tv_smfmac_f32_16x16x64_bf16 %[cs], %[sel], v[64:71], %[si]\n\t" \
  ".set k, 0\n\t.rept 16\n\tv_dot2c_f32_bf16 %[xq], v[xb+k], v[xb+k]\n\t.set k, k+1\n\t.endr\n\ts_nop 2"
#define STAGE(S, XREG, X) \
  asm volatile(STAGE_TEXT : XREG(X), "+{v[96:111]}"(accA), [xs] "+v"(xs), [cs] "+v"(cs), [xq] "+v"(xq) \
               : [s] "i"(S), [a0] "v"(xa[0]), [a1] "v"(xa[1]), [a2] "v"(xa[2]), [a3] "v"(xa[3]), [a4] "v"(xa[4]), [a5] "v"(xa[5]), [a6] "v"(xa[6]), \
                 [a7] "v"(xa[7]), [wa] "v"(wA1), [wb] "v"(wB1), [wc] "v"(wC1), [sel] "v"(sel), [si] "v"(selidx) : "memory", CLOB_W)
#define TL_RD ".set n, f+5\n\t.if n<64\n\t.set o, (n/32)*32768+((n/8)&1)*8192+(n&3)*1024\n\t.set b, 60+((n/16)&1)*2+((n/4)&1)\n\t.set w, 64+(n&7)*4\n\t" \
              "ds_read_b64_tr_b16 v[w:w+1], v[b] offset:o\n\tds_read_b64_tr_b16 v[w+2:w+3], v[b] offset:o+256\n\t.endif\n\t"
#define TL_MM(A) ".if f<59\n\ts_waitcnt lgkmcnt(10)\n\t.else\n\ts_waitcnt lgkmcnt(2*(63-f))\n\t.endif\n\t.set w, 64+(f&7)*4\n\t.set x, 128+(f&31)*4\n\t" \
              ".if (f&31)==0\n\tv_mfma_f32_32x32x16_bf16 v[" A "], v[w:w+3], v[x:x+3], 0\n\t.else\n\tv_mfma_f32_32x32x16_bf16 v[" A "], v[w:w+3], v[x:x+3], v[" A "]\n\t.endif\n\t"
#define TILE_SUB_TEXT \
  ".Ltile_sub:\n\t.set f, -5\n\t.rept 5\n\t" TL_RD ".set f, f+1\n\t.endr\n\t" \
  ".rept 32\n\t" TL_RD TL_MM("96:111") ".set f, f+1\n\t.endr\n\t" \
  "s_cmp_eq_u32 s46, 0\n\ts_cbranch_scc1 .Ltile_ret\n\ts_cmp_eq_u32 s46, 1\n\ts_cbranch_scc1 .Ltile_two\n\t" \
  "v_mov_b32 v58, %c0\n\tv_mov_b32 v59, 1\n\ts_waitcnt vmcnt(0)\n\ts_mov_b64 exec, 1\n\tds_add_u32 v58, v59\n\ts_mov_b64 exec, -1\n\t" \
  ".Ltile_two:\n\t.rept 32\n\t" TL_RD TL_MM("112:127") ".set f, f+1\n\t.endr\n\t" \
  ".Ltile_ret:\n\ts_waitcnt lgkmcnt(0)\n\ts_nop 9\n\ts_setpc_b64 s[44:45]\n\t"
#define TILES(LO, HI, MODE)   \
  asm volatile("s_cmp_eq_u32 s46, 3\n\ts_cbranch_scc1 1f\n\ts_call_b64 s[44:45], .Ltile_sub\n\t1:" : "+{v[96:111]}"(accA), "+{v[112:127]}"(accB) \
               : "{v[128:159]}"(X0), "{v[160:191]}"(X1), "{v[192:223]}"(X2), "{v[224:255]}"(X3), \
                 "{v[60:63]}"((u32x4){wA + (LO), wB + (LO), wA + (HI), wB + (HI)}), "{s46}"(MODE) : "memory", "scc", "s44", "s45", "v58", "v59", CLOB_W)

#define E(r) "v_exp_f32 v" #r ", v" #r "\n\t"
#define QK(Dst, Q) "v_mfma_f32_32x32x16_bf16 v[" Dst "], v[144:147], v[" Q "], v[112:127]\n\t"
#define PV(O, V, P) "v_smfmac_f32_32x32x32_bf16 v[" O "], v[" V "], v[" P "], v164\n\t"
#define UA(OP, VP1, QN, O, V0, X0, X1) E(16) E(17) PV(OP, VP1, "40:47") E(18) E(19) E(20) X0 QK("32:47", QN) X1 E(21) E(22) E(23) E(24) E(25) E(26) PV(O, V0, "16:23") E(27) E(28) E(29) E(30) E(31)
#define UB(OP, VP1, QN, O, V0, X0, X1) E(32) E(33) PV(OP, VP1, "24:31") E(34) E(35) E(36) X0 QK("16:31", QN) X1 E(37) E(38) E(39) E(40) E(41) E(42) PV(O, V0, "32:39") E(43) E(44) E(45) E(46) E(47)
#define TR(D, OFF) "ds_read_b64_tr_b16 v[" D "], v166 offset:" OFF "\n\t"
#define KRD(OFF) "ds_read_b128 v[144:147], v165 offset:" OFF "\n\t"
#define WT "s_waitcnt lgkmcnt(0)\n\t"
#define VADV "v_add_u32 v166, v167, v166\n\t"
#define TILE4(VP1, V0, V1, N0, N1, N2, N3, OK)                                          \
  UA("96:111", VP1, "132:135", "48:63", V0, VADV, TR(N0, "0") TR(N1, "128"))            \
  UB("48:63", V1, "136:139", "64:79", V0, "", TR(N2, "512") TR(N3, "640"))              \
  UA("64:79", V1, "140:143", "80:95", V0, "", KRD(OK))                                  \
  UB("80:95", V1, "128:131", "96:111", V0, WT, "")

__global__ __launch_bounds__(512, 2) void fused_kernel(const char* __restrict__ x, const char* __restrict__ wqkv, const char* __restrict__ wo,
                                                       const char* __restrict__ w1, const char* __restrict__ b1, const char* __restrict__ w2,
                                                       const char* __restrict__ b2, char* __restrict__ out, char* __restrict__ ws) {
  extern __shared__ __attribute__((aligned(16))) char smem[];
  const int lane = threadIdx.x & 63, wid = __builtin_amdgcn_readfirstlane(threadIdx.x >> 6);
  const int xcd = blockIdx.x & 7, idx = blockIdx.x >> 3;
  const int g = wid >> 2, tg = wid & 3;
  const int r = lane & 31, hh = lane >> 5, ch = (lane >> 4) & 1, q4 = (lane & 15) >> 2, p4 = lane & 3;
  const int p4s = ((p4 & 1) << 1) | (p4 >> 1);
  const int rs = (r & 19) | ((r & 4) << 1) | ((r & 8) >> 1);
  TS(0);
  u64 epoch;
  asm volatile("s_load_dwordx2 %0, %1, 0x0" : "=s"(epoch) : "s"(ws) : "memory");
  const int mt = (xcd >> 1) * 4 + (idx & 3), np = (xcd & 1) * 8 + (idx >> 2);
  const bool isk = np < 8, even = !(np & 1);
  const int f01 = even ? 2 * np : 32 + (np & ~1), f2 = even ? 2 * np + 2 : 48 + (np >> 1);
  const unsigned vq = (lane >> 2) * 3072 + (lane & 3) * 16, vf = (lane >> 2) * 3584 + (lane & 3) * 16;
  const char* xb = x + (size_t)(mt * 128 + 32 * tg) * 1024 + g * 128;
  unsigned vx[2];
#pragma unroll
  for (int i = 0; i < 2; ++i) vx[i] = (lane >> 3) * 1024 + (((lane & 7) ^ ((4 * i + (lane >> 4)) & 7)) << 4);
  auto dma_x = [&](int s) {
#pragma unroll
    for (int i = 0; i < 4; ++i) glds(xb + s * 256 + i * 8192, vx[i & 1], (s & 1) * 2 * U + g * U + (32 * tg + 8 * i) * 128);
  };
  const char* wab = wqkv + 1024 + np * 128 + (size_t)(16 * wid) * 3072;
  auto dma_wab = [&](int s) {
    const unsigned l = (4 + 2 * (s >> 1)) * U + (s & 1) * 8192 + wid * 1024;
    glds(wab + (size_t)s * 128 * 3072, vq, l);
    glds(wab + (size_t)s * 128 * 3072 + 64, vq, l + U);
  };
  dma_x(0); dma_wab(0); dma_x(1); dma_wab(1); dma_wab(2); dma_wab(3);
  unsigned bias0, bias1;
  {
    const unsigned bo = ((g ? f2 : f01) * 32 + rs) * 2, bo1 = (g && !even) ? bo : bo + 64;
    asm volatile("global_load_ushort %0, %2, %4\n\tglobal_load_ushort %1, %3, %4" : "=&v"(bias0), "=&v"(bias1) : "v"(bo), "v"(bo1), "s"(b1) : "memory");
  }
  if (wid == 4) {
    lds_wu(L_CNT + r * 4, 0u);
    lds_w128(L_CONST + (lane < 48 ? lane : 47) * 16, (u32x4){0x3f80u, 0u, 0u, 0u});
  }
  const unsigned ones = 0x3f803f80u;
  const bool selon = (lane & 3) < 2, selhi = (lane >> 2) & 1;
  const u32x4 sel = {selon && !selhi ? ones : 0u, selon && !selhi ? ones : 0u, selon && selhi ? ones : 0u, selon && selhi ? ones : 0u};
  const int selidx = (lane & 3) == 0 ? 0x4444 : 0xEEEE;
  const unsigned wl = (8 * hh + q4) * 64 + ch * 32 + p4s * 8;
  const unsigned wA = wl + g * 4096, wB = wl + (1 - g) * 4096;
  u32x32 X0, X1, X2, X3;
  f32x16 accA, accB;
  {
    f32x4 cs = {0.f, 0.f, 0.f, 0.f}, xs = {0.f, 0.f, 0.f, 0.f};
    float xq = 0.f;
    const int row = 32 * tg + r;
    unsigned xa[8];
#pragma unroll
    for (int k = 0; k < 8; ++k) xa[k] = (g ^ (k >> 2)) * U + row * 128 + (((2 * (k & 3) + hh) ^ ((row >> 1) & 7)) << 4);
    const unsigned wA1 = wA + (4 + g) * U, wB1 = wB + (4 + g) * U, wC1 = wl + tg * 2048 + (4 + g) * U;
    asm volatile("" : "={v[96:111]}"(accA), "={v[112:127]}"(accB));
    WAITV(12);
    BARRIER();
    asm volatile("" : "+s"(epoch));
    TS(1);
    STAGE(0, "={v[128:159]}", X0);
    WAITV(6);
    BARRIER();
    dma_x(2);
    STAGE(1, "={v[160:191]}", X1);
    WAITV(0);
    BARRIER();
    dma_x(3);
    {
      const char* wq = wqkv + np * 64 + (size_t)(16 * wid) * 3072;
#pragma unroll
      for (int i = 0; i < 4; ++i) glds(wq + (size_t)i * 128 * 3072, vq, (i < 2 ? 8 : 4) * U + (i & 1) * 8192 + wid * 1024);
    }
    STAGE(2, "={v[192:223]}", X2);
    WAITV(4);
    BARRIER();
    STAGE(3, "={v[224:255]}", X3);
    xq = swap_add(xq);
    const float c = cs[0] + cs[1], chi = rbf(c);
    lds_wf2(L_STAT + row * 16 + g * 8, (f32x2){xs[0] + xs[1], xq});
    const unsigned cw = pin(pk_bf16(chi, c - chi));
    lds_wu(L_AUG + g * 1024 + lane * 16 + tg * 4, hh ? 0u : cw);
  }
  BARRIER();
  TS(2);
  {
    const char* wp = w1 + f01 * 64 + (size_t)(16 * wid) * 3584;
#pragma unroll
    for (int i = 0; i < 4; ++i) {
      glds(wp + (size_t)i * 128 * 3584, vf, i * 8192 + wid * 1024);
      glds(wp + (size_t)i * 128 * 3584 + 64, vf, 2 * U + i * 8192 + wid * 1024);
    }
  }
  float rstd;
  u32x4 xaug;
  {
    const f32x4 st = lds_rf4(L_STAT + (32 * tg + r) * 16);
    const float mean = (st[0] + st[2]) * (1.f / 512.f);
    const float var = fmaxf((st[1] + st[3]) * (1.f / 512.f) - mean * mean, 0.f);
    const float sd = rbf(sqrtf(rbf(rbf(var) + 1e-5f)));
    rstd = 1.f / sd;
    const unsigned nm = pin(pk_bf16(-mean, -mean)), sw = pin(pk_bf16(sd, 0.f)), nz = hh ? 0u : nm;
    xaug = (u32x4){hh ? sw : nm, nz, nz, nz};
  }
  const int tt = mt * 4 + tg;
  auto finish = [&](f32x16 a, int aug, float f, bool relu2, u32x4& o0, u32x4& o1) {
    a = mfma32(lds_r128(L_AUG + aug * 1024 + lane * 16), xaug, a);
#pragma unroll
    for (int i = 0; i < 8; ++i) {
      float u = a[2 * i] * f, v = a[2 * i + 1] * f;
      if (relu2) { u = fmaxf(u, 0.f); v = fmaxf(v, 0.f); u *= u; v *= v; }
      if (i < 4) o0[i] = pk_bf16(u, v); else o1[i - 4] = pk_bf16(u, v);
    }
  };
  auto colsum = [&](unsigned lo, unsigned hi, int aug, unsigned bias) {
    const unsigned a = wl + (tg < 2 ? lo : hi) + (tg & 1) * 8192;
    f32x4 cs = {0.f, 0.f, 0.f, 0.f};
#pragma unroll
    for (int i = 0; i < 4; ++i) cs = fragsum(sel, selidx, lds_trf(a + i * 2048, 256), lds_trf(a + i * 2048 + 1024, 256), cs);
    const float c = cs[0] + cs[1], chi = rbf(c);
    const unsigned cw = pin(pk_bf16(chi, c - chi)), bw = pin(tg == 0 ? bias : 0u);
    lds_wu(L_AUG + aug * 1024 + lane * 16 + tg * 4, hh ? bw : cw);
  };
  {
    u32x4 o0, o1;
    finish(accA, g, rstd, false, o0, o1);
    const int head = (np & 7) * 4 + g * 2;
    char* kv = ws + WS_KV + ((size_t)head * 64 + tt) * 2048;
    const unsigned vo = isk ? lane * 16 : 1024 + r * 32 + hh * 16;
    st16_dev(kv, vo, o0);
    st16_dev(kv + 131072, vo, o1);
    if (isk) {
      const float n0 = swap_add(sq8(o0)), n1 = swap_add(sq8(o1));
      const unsigned m = umax32(__float_as_uint(hh ? n1 : n0));
      st4_dev2(ws + WS_KMAX + (head * 64 + tt) * 4, hh * 256, m);
    }
  }
  const u64 tag = [&] { u64 z = epoch + 0x9E3779B97F4A7C15ull; z = (z ^ (z >> 30)) * 0xBF58476D1CE4E5B9ull; z = (z ^ (z >> 27)) * 0x94D049BB133111EBull; return z ^ (z >> 31); }();
  WAITV(10);
  asm volatile("" : "+v"(bias0), "+v"(bias1));
  BARRIER();
  TS(3);
  u32x4 hp[4];
  const int head = xcd * 4 + (idx >> 3), qb = idx & 7;
  const int m2 = xcd * 4 + (idx & 3), n2 = idx >> 2;
  char* hb = ws + WS_H + (size_t)tt * 112 * 1024;
  const unsigned l16 = lane * 16;
  const int fr0 = blockIdx.x * 9 + tg * 3;
  TILES(8 * U, 4 * U, g ? 3 : 0);
  if (g) colsum(8 * U, 4 * U, 2, 0u);
  if (isk) WAITV(3); else WAITV(2);
  BARRIER();
  TS(4);
  TILES(0u, U, g ? 2 : 3);
  if (!g)
  {
    u32x4 o0, o1;
    finish(accA, 2, rstd * (0.25f * 1.4426950408889634f), false, o0, o1);
    char* qp = ws + WS_Q + ((size_t)(2 * np) * 64 + tt) * 1024;
    st16_dev(qp, lane * 16, o0);
    st16_dev(qp + 65536, lane * 16, o1);
    const char* wp = w1 + f2 * 64 + (size_t)(16 * tg) * 3584;
#pragma unroll 1
    for (int i = 0; i < 8; ++i) {
      glds(wp + (size_t)i * 64 * 3584, vf, 5 * U + i * 4096 + tg * 1024);
      if (even) glds(wp + (size_t)i * 64 * 3584 + 64, vf, 7 * U + i * 4096 + tg * 1024);
    }
    colsum(0, U, 0, bias0);
    colsum(2 * U, 3 * U, 1, bias1);
    if (even) WAITV(16); else WAITV(8);
    cnt_add<C_DRAIN>();
    TS(5);
    if (tg == 0) {
      cnt_wait<C_DRAIN, 8>();
      st8_dev1(ws + WS_FA, (np * 16 + mt) * 8, tag);
      WAITV(1);
    } else {
      WAITV(0);
    }
  }
  BARRIER();
  TS(6);
  TILES(5 * U, 6 * U, g ? 3 : even ? 1 : 0);
  if (g) {
    colsum(5 * U, 6 * U, 2, bias0);
    if (even) colsum(7 * U, 8 * U, 3, bias1);
    cnt_add<C_CS>();
    finish(accA, 0, rstd, true, hp[0], hp[1]);
    finish(accB, 1, rstd, true, hp[2], hp[3]);
    asm volatile("" : "+v"(hp[0]), "+v"(hp[1]), "+v"(hp[2]), "+v"(hp[3]));
    TS(7);
    {
      const unsigned fkv = pin((head >> 2) * 16 + ch * 128 + (lane & 15)), fq = pin((head >> 1) * 16 + 2 * qb + (lane & 1));
      const unsigned fi = hh ? fq : fkv;
      poll_flags(ws, WS_FA + fi * 8, tag);
    }
    TS(8);
    const char* kv = ws + WS_KV + (size_t)head * 131072;
    glds2_dev(ws + WS_Q + ((size_t)head * 64 + qb * 8 + 2 * tg) * 1024, l16, L_QF + tg * 2048);
    asm volatile("s_mov_b32 m0, %2\n\ts_nop 0\n\tglobal_load_lds_dword %0, %1 sc1" ::"v"(lane * 4), "s"(ws + WS_KMAX + head * 256), "s"(L_KM + tg * 256) : "memory");
    glds4_dev(kv + tg * 4096, l16, tg * 4096);
    glds4_dev(kv + 4 * U + tg * 4096, l16, 4 * U + tg * 4096);
#pragma unroll
    for (int i = 1; i < 4; ++i) glds4_dev(kv + i * U + tg * 4096, l16, i * U + tg * 4096);
    WAITV(12);
    cnt_add<C_KV1>();
    TS(9);
    cnt_wait<C_WDONE, 4>();
    cnt_wait<C_CS, 4>();
#pragma unroll
    for (int i = 5; i < 8; ++i) glds4_dev(kv + i * U + tg * 4096, l16, i * U + tg * 4096);
    if (tg < 3) {
#pragma unroll
      for (int i = 0; i < 3; ++i) {
        const int band = (fr0 + i) >> 4, nt = (fr0 + i) & 15;
        glds((band < 112 ? w2 + (size_t)band * 16384 : wo + (size_t)(band - 112) * 16384) + nt * 64, (lane >> 2) * 1024 + (lane & 3) * 16, 8 * U + tg * 4096 + i * 1024);
      }
      WAITV(3);
    } else {
      WAITV(0);
    }
    cnt_add<C_KV2>();
#pragma unroll
    for (int i = 0; i < 4; ++i) st16_dev(hb + (size_t)(f01 * 2 + i) * 1024, l16, hp[i]);
  } else {
    TS(7);
  cnt_add<C_WDONE>();
  cnt_wait<C_CS, 4>();
  finish(accA, 2, rstd, true, hp[0], hp[1]);
#pragma unroll
  for (int i = 0; i < 2; ++i) st16_dev(hb + (size_t)(f2 * 2 + i) * 1024, lane * 16, hp[i]);
  if (even) {
    finish(accB, 3, rstd, true, hp[2], hp[3]);
#pragma unroll
    for (int i = 2; i < 4; ++i) st16_dev(hb + (size_t)(f2 * 2 + i) * 1024, lane * 16, hp[i]);
  }
  }
  asm volatile("" ::: "memory");
  if (g) {
    if (tg < 3) {
      WAITV(4);
#pragma unroll
      for (int i = 0; i < 3; ++i) {
        const int band = (fr0 + i) >> 4, nt = (fr0 + i) & 15;
        const u32x4 v = lds_trf(8 * U + tg * 4096 + i * 1024 + wl, 256);
        st16_dev(ws + (band < 112 ? WS_W2 + (nt * 112 + band) * 1024 : WS_WO + (nt * 32 + band - 112) * 1024), l16, v);
      }
    }
    WAITV(0);
    cnt_add<C_HDRAIN>();
    if (tg == 0) {
      cnt_wait<C_HDRAIN, 8, 8>();
      st8_dev1(ws + WS_FB, blockIdx.x * 8, tag);
    }
    TS(10);
#pragma unroll 1
    for (int i = 0; i < 4; ++i) poll_flags<8>(ws, WS_FB + (i * 64 + lane) * 8, tag);
    TS(11);
    const auto rsrc = __builtin_amdgcn_make_buffer_rsrc(ws, 0, WS_END, 0x00020000);
    const unsigned ho[2] = {WS_H + ((m2 * 2 + (tg >> 1)) * 112 + tg * 28) * 1024, WS_H + ((m2 * 2 + 1 - (tg >> 1)) * 112 + tg * 28) * 1024};
    const unsigned wo2[2] = {WS_W2 + ((n2 * 2 + (tg & 1)) * 112 + tg * 28) * 1024, WS_W2 + ((n2 * 2 + 1 - (tg & 1)) * 112 + tg * 28) * 1024};
    constexpr int DEP = 9;
    u32x4 hf[DEP][2], wf[DEP][2];
    auto ld = [&](int s) {
#pragma unroll
      for (int u = 0; u < 2; ++u) {
        hf[s % DEP][u] = __builtin_amdgcn_raw_buffer_load_b128(rsrc, l16 + (s & 3) * 1024, ho[u] + (s & ~3) * 1024, 16);
        wf[s % DEP][u] = __builtin_amdgcn_raw_buffer_load_b128(rsrc, l16 + (s & 3) * 1024, wo2[u] + (s & ~3) * 1024, 16);
      }
    };
    cnt_wait<C_PROG, 4, 16>();
#pragma unroll
    for (int i = 0; i < 4; ++i)
      glds4_dev(ws + WS_WO + ((n2 * 2 + (i >> 1)) * 32 + tg * 8 + (i & 1) * 4) * 1024, l16, (i >> 1) * 4 * U + (tg * 8 + (i & 1) * 4) * 1024);
    WAITV(0);
    cnt_add<C_WO>();
    f32x16 fa[2][2];
#pragma unroll
    for (int a = 0; a < 2; ++a)
#pragma unroll
      for (int b = 0; b < 2; ++b)
#pragma unroll
        for (int i = 0; i < 16; ++i) fa[a][b][i] = 0.f;
#pragma unroll
    for (int s = 0; s < DEP; ++s) ld(s);
    SB();
#pragma unroll
    for (int s = 0; s < 28; ++s) {
      if (s == NBG) { asm volatile("s_barrier" ::: "memory"); TS(12); }
#pragma unroll
      for (int a = 0; a < 2; ++a)
#pragma unroll
        for (int b = 0; b < 2; ++b) fa[a][b] = mfma32(wf[s % DEP][b], hf[s % DEP][a], fa[a][b]);
      SB();
      if (s + DEP < 28) ld(s + DEP);
      SB();
    }
    TS(13);
    const int n0 = n2 * 64 + (tg & 1) * 32 + 8 * hh;
    const u32x4 bb0 = *(const u32x4*)(b2 + n0 * 2), bb1 = *(const u32x4*)(b2 + n0 * 2 + 32);
    auto fslot = [&](int dest, int d) { const int k = dest * 3 + d - 1; return (k < 8 ? 2 * U + k * 4096 : 6 * U + (k - 8) * 4096) + l16; };
#pragma unroll
    for (int d = 1; d < 4; ++d) {
      const f32x16 v = fa[d >> 1][d & 1];
      const unsigned a = fslot(tg ^ d, d);
#pragma unroll
      for (int j = 0; j < 4; ++j) lds_wf4(a + j * 1024, (f32x4){v[4 * j], v[4 * j + 1], v[4 * j + 2], v[4 * j + 3]});
    }
    cnt_add<C_FX>();
    cnt_wait<C_FX, 4>();
    f32x16 z = fa[0][0];
#pragma unroll
    for (int d = 1; d < 4; ++d)
#pragma unroll
      for (int j = 0; j < 4; ++j) {
        const f32x4 v = lds_rf4(fslot(tg, d) + j * 1024);
#pragma unroll
        for (int i = 0; i < 4; ++i) z[4 * j + i] += v[i];
      }
#pragma unroll
    for (int i = 0; i < 16; ++i) {
      const unsigned bw = i < 8 ? bb0[i >> 1] : bb1[(i - 8) >> 1];
      const float y = z[i] + __uint_as_float((i & 1) ? (bw & 0xffff0000u) : (bw << 16));
      z[i] = y * __builtin_amdgcn_rcpf(1.f + __builtin_amdgcn_exp2f(y * -1.4426950408889634f));
    }
#pragma unroll
    for (int j = 0; j < 4; ++j) lds_wf4(7 * U + tg * 4096 + j * 1024 + l16, (f32x4){z[4 * j], z[4 * j + 1], z[4 * j + 2], z[4 * j + 3]});
    cnt_add<C_G>();
    TS(14);
    TSFLUSH();
    return;
  }
  TS(8);
  cnt_wait<C_KV1, 4>();
  TS(9);
  const int qg = tg & 1, kh = tg >> 1;
  u32x4 qf[4];
#pragma unroll
  for (int u = 0; u < 4; ++u) qf[u] = lds_r128(L_QF + (4 * qg + (u ^ (2 * kh))) * 1024 + lane * 16);
  unsigned kmu = umax32(__float_as_uint(lds_rf(L_KM + tg * 256 + lane * 4)));
  { auto t2 = __builtin_amdgcn_permlane32_swap(kmu, kmu, false, false); kmu = umax(t2[0], t2[1]); }
  const unsigned qn = umax(umax(__float_as_uint(swap_add(sq8(qf[0]))), __float_as_uint(swap_add(sq8(qf[1])))),
                           umax(__float_as_uint(swap_add(sq8(qf[2]))), __float_as_uint(swap_add(sq8(qf[3])))));
  const float bound = sqrtf(__uint_as_float(qn) * __uint_as_float(kmu)) * 1.001f;
  f32x16 o[4];
  float mx[4];
  unsigned ka = kh * 4 * U + lane * 16;
  const unsigned vac = pin(L_CONST + (p4 >> 1) * 8), vad = pin(kh * 4 * U + 1024 + (8 * hh + q4) * 32 + p4s * 8);
  unsigned va = ch ? vac : vad;
  const unsigned vstep = ch ? 0u : 2048u;
#pragma unroll
  for (int u = 0; u < 4; ++u)
#pragma unroll
    for (int i = 0; i < 16; ++i) o[u][i] = 0.f;
  if (__all(bound <= BOUND_LIMIT)) {
    f32x16 cneg;
#pragma unroll
    for (int i = 0; i < 16; ++i) cneg[i] = -bound;
    const u32x8 q01 = __builtin_shufflevector(qf[0], qf[1], 0, 1, 2, 3, 4, 5, 6, 7), q23 = __builtin_shufflevector(qf[2], qf[3], 0, 1, 2, 3, 4, 5, 6, 7);
    asm volatile(
        "v_mov_b32 v164, 0xdddd\n\t"
        KRD("0") TR("148:149", "0") TR("150:151", "128") TR("152:153", "512") TR("154:155", "640")
        "v_mov_b32 v40, 0\n\tv_mov_b32 v41, 0\n\tv_mov_b32 v42, 0\n\tv_mov_b32 v43, 0\n\t"
        "v_mov_b32 v44, 0\n\tv_mov_b32 v45, 0\n\tv_mov_b32 v46, 0\n\tv_mov_b32 v47, 0\n\t"
        "v_mov_b32 v160, 0\n\tv_mov_b32 v161, 0\n\tv_mov_b32 v162, 0\n\tv_mov_b32 v163, 0\n\t"
        "v_mov_b32 v168, %[cnt]\n\tv_mov_b32 v169, 1\n\t"
        WT
        QK("16:31", "128:131")
        "s_mov_b32 s40, 0\n\t"
        "s_mov_b32 s41, 3\n\t"
        "s_nop 10\n\t"
        "1:\n\t"
        TILE4("160:163", "148:151", "152:155", "156:157", "158:159", "160:161", "162:163", "2048")
        TILE4("152:155", "156:159", "160:163", "148:149", "150:151", "152:153", "154:155", "4096")
        "v_add_u32 v165, 0x1000, v165\n\t"
        "s_add_u32 s40, s40, 1\n\t"
        "s_cmp_lt_u32 s40, s41\n\t"
        "s_cbranch_scc1 1b\n\t"
        "s_cmp_eq_u32 s40, 16\n\t"
        "s_cbranch_scc1 4f\n\t"
        "s_cmp_eq_u32 s40, 3\n\t"
        "s_cbranch_scc0 3f\n\t"
        "s_waitcnt vmcnt(0)\n\t"
        "s_mov_b64 exec, 1\n\t"
        "ds_add_u32 v168, v169 offset:%[chd]\n\t"
        "s_mov_b64 exec, -1\n\t"
        "s_mov_b32 s41, 0\n\t"
        "2:\n\t"
        "ds_read_b32 v170, v168 offset:%[ckv]\n\t"
        "s_add_u32 s41, s41, 1\n\t"
        "s_waitcnt lgkmcnt(0)\n\t"
        "v_readfirstlane_b32 s42, v170\n\t"
        "s_bitcmp1_b32 s41, 22\n\t"
        "s_cselect_b32 s42, 4, s42\n\t"
        "s_cmp_lt_u32 s42, 4\n\t"
        "s_cbranch_scc1 2b\n\t"
        "s_mov_b32 s41, 8\n\t"
        "s_branch 1b\n\t"
        "3:\n\t"
        "s_mov_b64 exec, 1\n\t"
        "ds_add_u32 v168, v169 offset:%[cpr]\n\t"
        "s_mov_b64 exec, -1\n\t"
        "s_mov_b32 s41, 16\n\t"
        "s_branch 1b\n\t"
        "4:\n\t"
        PV("96:111", "160:163", "40:47")
        "s_nop 15\n\t"
        : "+{v[48:63]}"(o[0]), "+{v[64:79]}"(o[1]), "+{v[80:95]}"(o[2]), "+{v[96:111]}"(o[3]), "+{v165}"(ka), "+{v166}"(va)
        : "{v[112:127]}"(cneg), "{v[128:135]}"(q01), "{v[136:143]}"(q23), "{v167}"(vstep), [cnt] "i"(L_CNT), [chd] "i"(4 * C_HDRAIN),
          [ckv] "i"(4 * C_KV2), [cpr] "i"(4 * C_PROG)
        : "memory", "scc", "s40", "s41", "s42", "v16", "v17", "v18", "v19", "v20", "v21", "v22", "v23", "v24", "v25", "v26", "v27", "v28", "v29", "v30",
          "v31", "v32", "v33", "v34", "v35", "v36", "v37", "v38", "v39", "v40", "v41", "v42", "v43", "v44", "v45", "v46", "v47", "v144", "v145",
          "v146", "v147", "v148", "v149", "v150", "v151", "v152", "v153", "v154", "v155", "v156", "v157", "v158", "v159", "v160", "v161", "v162",
          "v163", "v164", "v168", "v169", "v170");
#pragma unroll
    for (int u = 0; u < 4; ++u) mx[u] = bound;
  } else {
    WAITV(0);
    cnt_add<C_HDRAIN>();
    cnt_wait<C_KV2, 4>();
    f32x16 z;
#pragma unroll
    for (int i = 0; i < 16; ++i) z[i] = 0.f;
#pragma unroll
    for (int u = 0; u < 4; ++u) mx[u] = -3.0e38f;
#pragma unroll 1
    for (int t = 0; t < 32; ++t) {
      if (t == 16) cnt_add<C_PROG>();
      const u32x4 kf = lds_r128(ka), v0 = lds_trf(va, 128), v1 = lds_trf(va + 512, 128);
      ka += 2048; va += vstep;
#pragma unroll
      for (int u = 0; u < 4; ++u) {
        f32x16 s = mfma32(kf, qf[u], z);
        float m = s[0];
#pragma unroll
        for (int i = 1; i < 16; ++i) m = fmaxf(m, s[i]);
        m = fmaxf(swap_max(m), mx[u]);
        const float al = __builtin_amdgcn_exp2f(mx[u] - m);
        mx[u] = m;
#pragma unroll
        for (int i = 0; i < 16; ++i) { o[u][i] *= al; s[i] = __builtin_amdgcn_exp2f(s[i] - m); }
        const f32x8 plo = __builtin_shufflevector(s, s, 0, 1, 2, 3, 4, 5, 6, 7), phi = __builtin_shufflevector(s, s, 8, 9, 10, 11, 12, 13, 14, 15);
        o[u] = __builtin_amdgcn_smfmac_f32_32x32x32_bf16(__builtin_bit_cast(bf16x8, v0), __builtin_bit_cast(bf16x16, plo), o[u], 0xdddd, 0, 0);
        o[u] = __builtin_amdgcn_smfmac_f32_32x32x32_bf16(__builtin_bit_cast(bf16x8, v1), __builtin_bit_cast(bf16x16, phi), o[u], 0xdddd, 0, 0);
      }
    }
  }
  TS(10);
  cnt_wait<C_WO, 4>();
#pragma unroll
  for (int u = 0; u < 2; ++u) {
    const f32x16 v = o[2 + u];
    lds_wf4(8 * U + tg * 4096 + u * 2048 + l16, (f32x4){v[0], v[1], v[2], v[3]});
    lds_wf4(8 * U + tg * 4096 + u * 2048 + 1024 + l16, (f32x4){v[4], v[5], v[6], v[7]});
    lds_wf(L_STAT + tg * 512 + u * 256 + lane * 4, hh ? mx[2 + u] : v[8]);
  }
  BARRIER();
  const int mo = qb * 4 + 2 * qg + kh;
#pragma unroll
  for (int u = 0; u < 2; ++u) {
    const unsigned pa = 8 * U + (tg ^ 2) * 4096 + u * 2048 + l16, za = L_STAT + (tg ^ 2) * 512 + u * 256 + r * 4;
    const f32x4 a = lds_rf4(pa), b = lds_rf4(pa + 1024);
    const float lp = lds_rf(za), mp = lds_rf(za + 128);
    const float mn = fmaxf(mx[u], mp);
    const float f0 = __builtin_amdgcn_exp2f(mx[u] - mn), f1 = __builtin_amdgcn_exp2f(mp - mn);
    const float inv = __builtin_amdgcn_rcpf(o[u][8] * f0 + lp * f1);
    const float g0 = f0 * inv, g1 = f1 * inv;
    u32x4 w;
    w[0] = pk_bf16(o[u][0] * g0 + a[0] * g1, o[u][1] * g0 + a[1] * g1);
    w[1] = pk_bf16(o[u][2] * g0 + a[2] * g1, o[u][3] * g0 + a[3] * g1);
    w[2] = pk_bf16(o[u][4] * g0 + b[0] * g1, o[u][5] * g0 + b[1] * g1);
    w[3] = pk_bf16(o[u][6] * g0 + b[2] * g1, o[u][7] * g0 + b[3] * g1);
    st16_dev(ws + WS_O + ((size_t)(mo * 2 + u) * 32 + head) * 1024, l16, w);
  }
  WAITV(0);
  st8_dev1(ws + WS_FC, (mo * 32 + head) * 8, tag);
  TS(11);
  const size_t eo = ((size_t)(m2 * 64 + (tg >> 1) * 32 + r) * 512 + n2 * 64 + (tg & 1) * 32 + 8 * hh) * 2;
  const u32x4 xv0 = *(const u32x4*)(x + eo), xv1 = *(const u32x4*)(x + eo + 32);
  u32x4 wf[8][2];
#pragma unroll
  for (int s = 0; s < 8; ++s)
#pragma unroll
    for (int b = 0; b < 2; ++b) wf[s][b] = lds_r128(((b ^ (tg & 1)) * 4 * U) + (tg * 8 + s) * 1024 + l16);
  poll_flags(ws, WS_FC + (m2 * 32 + tg * 8 + (lane & 7)) * 8, tag);
  TS(12);
  const auto rsrc = __builtin_amdgcn_make_buffer_rsrc(ws, 0, WS_END, 0x00020000);
  u32x4 of[8][2];
#pragma unroll
  for (int s = 0; s < 8; ++s)
#pragma unroll
    for (int a = 0; a < 2; ++a)
      of[s][a] = __builtin_amdgcn_raw_buffer_load_b128(rsrc, l16 + (s & 3) * 1024, WS_O + ((m2 * 2 + (a ^ (tg >> 1))) * 32 + tg * 8 + (s & 4)) * 1024, 16);
  SB();
  f32x16 sa[2][2];
#pragma unroll
  for (int a = 0; a < 2; ++a)
#pragma unroll
    for (int b = 0; b < 2; ++b)
#pragma unroll
      for (int i = 0; i < 16; ++i) sa[a][b][i] = 0.f;
#pragma unroll
  for (int s = 0; s < 8; ++s)
#pragma unroll
    for (int a = 0; a < 2; ++a)
#pragma unroll
      for (int b = 0; b < 2; ++b) sa[a][b] = mfma32(wf[s][b], of[s][a], sa[a][b]);
  TS(13);
  auto aslot = [&](int src, int d) { return (d == 3 ? 4 * U : (d - 1) * 4096) + src * 8192 + l16; };
#pragma unroll
  for (int d = 1; d < 4; ++d) {
    const f32x16 v = sa[d >> 1][d & 1];
#pragma unroll
    for (int j = 0; j < 4; ++j) lds_wf4(aslot(tg, d) + j * 1024, (f32x4){v[4 * j], v[4 * j + 1], v[4 * j + 2], v[4 * j + 3]});
  }
  cnt_add<C_AX>();
  cnt_wait<C_AX, 4>();
  f32x16 sv = sa[0][0];
#pragma unroll
  for (int d = 1; d < 4; ++d)
#pragma unroll
    for (int j = 0; j < 4; ++j) {
      const f32x4 v = lds_rf4(aslot(tg ^ d, d) + j * 1024);
#pragma unroll
      for (int i = 0; i < 4; ++i) sv[4 * j + i] += v[i];
    }
  cnt_wait<C_G, 4>();
  TS(14);
  u32x4 res[2];
#pragma unroll
  for (int j = 0; j < 4; ++j) {
    const f32x4 gv = lds_rf4(7 * U + tg * 4096 + j * 1024 + l16);
#pragma unroll
    for (int i = 0; i < 2; ++i) {
      const unsigned xw = j < 2 ? xv0[2 * j + i] : xv1[2 * (j - 2) + i];
      res[j >> 1][2 * (j & 1) + i] = pk_bf16(__uint_as_float(xw << 16) + sv[4 * j + 2 * i] * gv[2 * i],
                                             __uint_as_float(xw & 0xffff0000u) + sv[4 * j + 2 * i + 1] * gv[2 * i + 1]);
    }
  }
  *(u32x4*)(out + eo) = res[0];
  *(u32x4*)(out + eo + 32) = res[1];
  if (blockIdx.x == 0 && wid == 0) st8_dev1(ws, 0u, tag);
  TS(15);
  TSFLUSH();
  asm volatile("s_endpgm\n\t" TILE_SUB_TEXT :: "i"(L_CNT + 4 * C_DRAIN) : "memory");
}

extern "C" void kernel_launch(void* const* d_in, const int* in_sizes, int n_in, void* d_out, int out_size, void* d_ws, size_t ws_size,
                              hipStream_t stream) {
  (void)hipFuncSetAttribute((const void*)fused_kernel, hipFuncAttributeMaxDynamicSharedMemorySize, L_TOTAL);
  fused_kernel<<<256, 512, L_TOTAL, stream>>>((const char*)d_in[0], (const char*)d_in[1], (const char*)d_in[2], (const char*)d_in[4],
                                              (const char*)d_in[5], (const char*)d_in[6], (const char*)d_in[7], (char*)d_out, (char*)d_ws);
}
```

```cpp
#include <hip/hip_runtime.h>
#include <hip/hip_bf16.h>

typedef __attribute__((ext_vector_type(8))) __bf16 bf16x8;
typedef __attribute__((ext_vector_type(16))) __bf16 bf16x16;
typedef __attribute__((ext_vector_type(2))) __bf16 bf16x2;
typedef __attribute__((ext_vector_type(16))) float f32x16;
typedef __attribute__((ext_vector_type(8))) float f32x8;
typedef __attribute__((ext_vector_type(4))) float f32x4;
typedef __attribute__((ext_vector_type(2))) float f32x2;
typedef __attribute__((ext_vector_type(4))) unsigned u32x4;
typedef __attribute__((ext_vector_type(8))) unsigned u32x8;
typedef __attribute__((ext_vector_type(32))) unsigned u32x32;
typedef __attribute__((ext_vector_type(2))) unsigned u32x2;
typedef __attribute__((ext_vector_type(4))) short s16x4;
typedef unsigned long long u64;
#define LDS_AS __attribute__((address_space(3)))
#define GLB_AS __attribute__((address_space(1)))
#define DI __device__ __forceinline__

constexpr unsigned WS_FLAG = 0;
constexpr unsigned WS_FA = 1024;
constexpr unsigned WS_FB = 4096;
constexpr unsigned WS_FC = 8192;
constexpr unsigned WS_KMAX = 16384;
constexpr unsigned WS_Q = 32768;
constexpr unsigned WS_KV = WS_Q + (2u << 20);
constexpr unsigned WS_H = WS_KV + (4u << 20);
constexpr unsigned WS_W2 = WS_H + (7u << 20);
constexpr unsigned WS_WO = WS_W2 + 1792u * 1024u;
constexpr unsigned WS_O = WS_WO + 512u * 1024u;
constexpr unsigned WS_END = WS_O + (2u << 20);

constexpr unsigned U = 16384;
constexpr unsigned L_MISC = 9 * U, L_AUG = L_MISC, L_STAT = L_MISC + 4096, L_CNT = L_MISC + 6144, L_CONST = L_CNT + 128,
                   L_QF = L_MISC + 7168, L_KM = L_QF + 8192, L_TOTAL = 10 * U;
enum { C_DRAIN = 0, C_CS, C_KV1, C_KV2, C_HDRAIN, C_PROG  , C_WO, C_FX, C_G, C_AX, C_FB };
constexpr float BOUND_LIMIT = 40.f;
#define NBG 8

#define STAMPS 1
constexpr size_t WS_STAMP = 64u << 20;
#define TS(i) do { u64 t_; asm volatile("s_memrealtime %1\n\ts_waitcnt lgkmcnt(0)\n\tv_writelane_b32 %0, %1, %c2" : "+v"(tsv), "=&s"(*(unsigned*)&t_) : "i"(i) : "memory"); } while (0)
#undef TS
#define TS(i) asm volatile("s_memrealtime s[98:99]\n\ts_waitcnt lgkmcnt(0)\n\tv_writelane_b32 %0, s98, %c1" : "+v"(tsv) : "i"(i) : "memory", "s98", "s99")
#define TSFLUSH() do { if (wid == 0 && blockIdx.x == PB) { unsigned t0 = __builtin_amdgcn_readlane(tsv, 0), ta = __builtin_amdgcn_readlane(tsv, 0), tb = __builtin_amdgcn_readlane(tsv, 0); unsigned tgt = 6000 + 0 * (tb - ta); while ((unsigned)__builtin_amdgcn_s_memrealtime() - t0 < tgt) __builtin_amdgcn_s_sleep(1); } } while (0)
#define PB 77
#include <cstdio>
#include <algorithm>
#include <vector>
static void print_stamps(const unsigned long long* h64) {
  const unsigned* h = (const unsigned*)h64;
  unsigned t0 = ~0u; for (int b = 0; b < 256; ++b) for (int w = 0; w < 8; ++w) { unsigned v = h[(b * 8 + w) * 64]; if (v && v < t0) t0 = v; }
  for (int w = 0; w < 8; w += 4) { printf("group %d: idx: min med max | median delta from previous stamp\n", w / 4); int prev = -1;
    for (int i = 0; i < 64; ++i) { std::vector<double> v, d; for (int b = 0; b < 256; ++b) for (int ww = w; ww < w + 4; ++ww) { unsigned x = h[(b * 8 + ww) * 64 + i]; if (x && x - t0 < 100000) { v.push_back((x - t0) / 100.0); if (prev >= 0 && h[(b * 8 + ww) * 64 + prev]) d.push_back((x - h[(b * 8 + ww) * 64 + prev]) / 100.0); } }
      if (v.empty()) continue; std::sort(v.begin(), v.end()); std::sort(d.begin(), d.end());
      printf(" [%2d] %5.2f %5.2f %5.2f | %5.2f%s", i, v[0], v[v.size() / 2], v.back(), d.empty() ? 0.0 : d[d.size() / 2], (i % 3 == 2) ? "\n" : "  "); prev = i; }
    printf("\n"); }
}

#define f2u(x) __builtin_bit_cast(unsigned, (float)(x))
#define u2f(x) __builtin_bit_cast(float, (unsigned)(x))
#define fmaxf __builtin_fmaxf
DI f32x16 mfma32(u32x4 a, u32x4 b, f32x16 c) {
  return __builtin_amdgcn_mfma_f32_32x32x16_bf16(__builtin_bit_cast(bf16x8, a), __builtin_bit_cast(bf16x8, b), c, 0, 0, 0);
}
DI f32x4 fragsum(u32x4 sel, int selidx, u32x4 f0, u32x4 f1, f32x4 c) {
  const u32x8 b = __builtin_shufflevector(f0, f1, 0, 1, 2, 3, 4, 5, 6, 7);
  return __builtin_amdgcn_smfmac_f32_16x16x64_bf16(__builtin_bit_cast(bf16x8, sel), __builtin_bit_cast(bf16x16, b), c, selidx, 0, 0);
}
DI unsigned pk_bf16(float lo, float hi) {
  f32x2 f = {lo, hi};
  return __builtin_bit_cast(unsigned, __builtin_convertvector(f, bf16x2));
}
DI float rbf(float x) { return (float)(__bf16)x; }
DI float dot2(unsigned a, unsigned b, float c) {
  return __builtin_amdgcn_fdot2_f32_bf16(__builtin_bit_cast(bf16x2, a), __builtin_bit_cast(bf16x2, b), c, false);
}
DI float sq8(u32x4 v) { return dot2(v[0], v[0], dot2(v[1], v[1], dot2(v[2], v[2], dot2(v[3], v[3], 0.f)))); }
DI u32x4 lds_r128(unsigned addr) { return *(const LDS_AS u32x4*)(uintptr_t)addr; }
DI f32x4 lds_rf4(unsigned addr) { return *(const LDS_AS f32x4*)(uintptr_t)addr; }
DI float lds_rf(unsigned addr) { return *(const LDS_AS float*)(uintptr_t)addr; }
DI void lds_wu(unsigned addr, unsigned v) { *(LDS_AS unsigned*)(uintptr_t)addr = v; }
DI void lds_wf(unsigned addr, float v) { *(LDS_AS float*)(uintptr_t)addr = v; }
DI void lds_wf2(unsigned addr, f32x2 v) { *(LDS_AS f32x2*)(uintptr_t)addr = v; }
DI void lds_wf4(unsigned addr, f32x4 v) { *(LDS_AS f32x4*)(uintptr_t)addr = v; }
DI void lds_w128(unsigned addr, u32x4 v) { *(LDS_AS u32x4*)(uintptr_t)addr = v; }
DI u32x2 lds_tr(unsigned addr) {
  return __builtin_bit_cast(u32x2, __builtin_amdgcn_ds_read_tr16_b64_v4i16((LDS_AS s16x4*)(uintptr_t)addr));
}
DI u32x4 lds_trf(unsigned addr, unsigned step) {
  const u32x2 lo = lds_tr(addr), hi = lds_tr(addr + step);
  return (u32x4){lo[0], lo[1], hi[0], hi[1]};
}
DI float swap_add(float v) {
  auto r = __builtin_amdgcn_permlane32_swap(f2u(v), f2u(v), false, false);
  return u2f(r[0]) + u2f(r[1]);
}
DI float swap_max(float v) {
  auto r = __builtin_amdgcn_permlane32_swap(f2u(v), f2u(v), false, false);
  return fmaxf(u2f(r[0]), u2f(r[1]));
}
DI unsigned umax(unsigned a, unsigned b) { return a > b ? a : b; }
DI unsigned umax32(unsigned m) {
  m = umax(m, (unsigned)__builtin_amdgcn_update_dpp(0, (int)m, 0xB1, 0xf, 0xf, true));
  m = umax(m, (unsigned)__builtin_amdgcn_update_dpp(0, (int)m, 0x4E, 0xf, 0xf, true));
  m = umax(m, (unsigned)__builtin_amdgcn_update_dpp(0, (int)m, 0x141, 0xf, 0xf, true));
  m = umax(m, (unsigned)__builtin_amdgcn_update_dpp(0, (int)m, 0x140, 0xf, 0xf, true));
  auto r = __builtin_amdgcn_permlane16_swap(m, m, false, false);
  return umax(r[0], r[1]);
}
DI void glds(const void* base, unsigned voff, unsigned lds) {
  asm volatile("s_mov_b32 m0, %2\n\ts_nop 0\n\tglobal_load_lds_dwordx4 %0, %1" ::"v"(voff), "s"(base), "s"(lds) : "memory");
}
DI void glds4_dev(const void* base, unsigned voff, unsigned lds) {
  asm volatile("s_mov_b32 m0, %2\n\ts_nop 0\n\tglobal_load_lds_dwordx4 %0, %1 sc1\n\tglobal_load_lds_dwordx4 %0, %1 offset:1024 sc1\n\t"
               "global_load_lds_dwordx4 %0, %1 offset:2048 sc1\n\tglobal_load_lds_dwordx4 %0, %1 offset:3072 sc1" ::"v"(voff), "s"(base), "s"(lds) : "memory");
}
DI void glds2_dev(const void* base, unsigned voff, unsigned lds) {
  asm volatile("s_mov_b32 m0, %2\n\ts_nop 0\n\tglobal_load_lds_dwordx4 %0, %1 sc1\n\tglobal_load_lds_dwordx4 %0, %1 offset:1024 sc1" ::"v"(voff), "s"(base), "s"(lds) : "memory");
}
DI void st16_dev(void* base, unsigned voff, u32x4 v) {
  asm volatile("global_store_dwordx4 %0, %1, %2 sc1\n\ts_nop 1" ::"v"(voff), "v"(v), "s"(base) : "memory");
}
DI void st4_dev2(void* base, unsigned voff, unsigned v) {
  asm volatile("s_mov_b64 exec, %3\n\tglobal_store_dword %0, %1, %2 sc1\n\ts_mov_b64 exec, -1" ::"v"(voff), "v"(v), "s"(base), "s"(0x100000001ull) : "memory");
}
DI void st8_dev1(void* base, unsigned voff, u64 v) {
  asm volatile("s_mov_b64 exec, 1\n\tglobal_store_dwordx2 %0, %1, %2 sc1\n\ts_mov_b64 exec, -1" ::"v"(voff), "v"(v), "s"(base) : "memory");
}
DI unsigned pin(unsigned v) { asm volatile("" : "+v"(v)); return v; }
#define BARRIER() asm volatile("s_waitcnt lgkmcnt(0)\n\ts_barrier" ::: "memory")
#define WAITV(n) asm volatile("s_waitcnt vmcnt(" #n ")" ::: "memory")
#define WAITL() asm volatile("s_waitcnt lgkmcnt(0)" ::: "memory")
#define SB() __builtin_amdgcn_sched_barrier(0)
template <int C> DI void cnt_add() {
  asm volatile("s_waitcnt lgkmcnt(0)\n\ts_mov_b64 exec, 1\n\tds_add_u32 %0, %1 offset:%c2\n\ts_mov_b64 exec, -1" ::"v"(L_CNT), "v"(1u), "i"(4 * C) : "memory");
}
template <int C, int N, int SLEEP = 1> DI void cnt_wait() {
  unsigned t, i, v;
  asm volatile("s_mov_b32 %1, 0\n\ts_branch 2f\n\t1:\n\ts_sleep %c6\n\t2:\n\tds_read_b32 %0, %3 offset:%c4\n\ts_add_u32 %1, %1, 1\n\ts_waitcnt lgkmcnt(0)\n\t"
               "v_readfirstlane_b32 %2, %0\n\ts_bitcmp1_b32 %1, 20\n\ts_cselect_b32 %2, %c5, %2\n\ts_cmp_lt_u32 %2, %c5\n\ts_cbranch_scc1 1b"
               : "=&v"(t), "=&s"(i), "=&s"(v) : "v"(L_CNT), "i"(4 * C), "i"(N), "i"(SLEEP) : "memory", "scc");
}
template <int SLEEP = 2> DI void poll_flags(const char* ws, unsigned voff, u64 tag) {
  u64 v; unsigned i;
  asm volatile("s_mov_b32 %1, 0\n\ts_branch 2f\n\t1:\n\ts_sleep %c5\n\t2:\n\tglobal_load_dwordx2 %0, %2, %3 sc1\n\ts_add_u32 %1, %1, 1\n\ts_waitcnt vmcnt(0)\n\t"
               "v_cmp_ne_u64 vcc, %4, %0\n\ts_bitcmp1_b32 %1, 17\n\ts_cbranch_scc1 3f\n\ts_cbranch_vccnz 1b\n\t3:"
               : "=&v"(v), "=&s"(i) : "v"(voff), "s"(ws), "s"(tag), "i"(SLEEP) : "memory", "scc", "vcc");
}

#define CLOB_W "v56", "v57", "v58", "v59", "v60", "v61", "v62", "v63", "v64", "v65", "v66", "v67", "v68", "v69", "v70", "v71", "v72", "v73", "v74", "v75", \
               "v76", "v77", "v78", "v79", "v80", "v81", "v82", "v83", "v84", "v85", "v86", "v87", "v88", "v89", "v90", "v91", "v92", "v93", "v94", "v95"
#define P1_DX(J) "s_add_u32 m0, s57, (t&3)*16384+1024*" #J "\n\ts_nop 0\n\tglobal_load_lds_dwordx4 v[34+" #J "], s[48:49]\n\t"
#define P1_DX1 P1_DX(1) "s_add_u32 s48, s48, 128\n\ts_addc_u32 s49, s49, 0\n\t"
#define P1_DW "s_add_u32 m0, s56, 65536+(t&3)*8192\n\ts_nop 0\n\tglobal_load_lds_dwordx4 v36, s[50:51]\n\ts_add_u32 s50, s50, 64*3072\n\ts_addc_u32 s51, s51, 0\n\t"
#define P1_DQ ".if (t&1)==0\n\ts_add_u32 m0, s56, 98304+(t/2)*8192\n\ts_nop 0\n\tglobal_load_lds_dwordx4 v37, s[52:53]\n\ts_add_u32 s52, s52, 128*3072\n\ts_addc_u32 s53, s53, 0\n\t.endif\n\t"
#define P1_DF(J) "s_add_u32 m0, s57, (t&3)*16384+1024*" #J "\n\ts_nop 0\n\tglobal_load_lds_dwordx4 v[38+" #J "], s[54:55]\n\t"
#define P1_DF1 P1_DF(1) "s_add_u32 s54, s54, 128*3584\n\ts_addc_u32 s55, s55, 0\n\t"
#define P1_D0 ".if t<8\n\t" P1_DX(0) ".elseif t<11\n\t" P1_DF(0) ".endif\n\t"
#define P1_D1 ".if t<8\n\t" P1_DX1 ".elseif t<11\n\t" P1_DF1 ".endif\n\t"
#define P1_D2 ".if t<8\n\t" P1_DW ".endif\n\t"
#define P1_D3 ".if t<8\n\t" P1_DQ ".endif\n\t"
#define P1_WR(I, Z) ".if " #I "==0\n\t.set w, 64+4*" Z "\n\t.else\n\t.set w, 68+12*" Z "+4*" #I "\n\t.endif\n\t"
#define P1_RD(I) ".if s<8\n\t" P1_WR(I, "(s&1)") "ds_read_b128 v[128+16*s+4*" #I ":131+16*s+4*" #I "], v[24+" #I "] offset:(s&3)*16384\n\t" \
                 "ds_read_b64_tr_b16 v[w:w+1], v[28+" #I "] offset:(s&3)*8192\n\tds_read_b64_tr_b16 v[w+2:w+3], v[28+" #I "] offset:(s&3)*8192+512\n\t.endif\n\t"
#define P1_RQ(I) ".if s<8\n\t.set w, 48+8*" #I "+4*(s&1)\n\tds_read_b64_tr_b16 v[w:w+1], v[32+" #I "] offset:s*4096\n\tds_read_b64_tr_b16 v[w+2:w+3], v[32+" #I "] offset:s*4096+256\n\t.endif\n\t"
#define P1_MM(I) ".if s>0\n\t" P1_WR(I, "(p&1)") ".set xr, 128+16*p+4*" #I "\n\t.if (p==0)&&(" #I "==1)\n\tv_mfma_f32_32x32x16_bf16 v[96:111], v[w:w+3], v[xr:xr+3], 0\n\t.else\n\t" \
                 "v_mfma_f32_32x32x16_bf16 v[96:111], v[w:w+3], v[xr:xr+3], v[96:111]\n\t.endif\n\t.endif\n\t"
#define P1_MQ(I) ".if s>0\n\t.set w, 48+8*" #I "+4*(p&1)\n\t.set xr, 128+16*p+4*" #I "\n\t.if (p+" #I ")==0\n\tv_mfma_f32_32x32x16_bf16 v[112:127], v[w:w+3], v[xr:xr+3], 0\n\t.else\n\t" \
                 "v_mfma_f32_32x32x16_bf16 v[112:127], v[w:w+3], v[xr:xr+3], v[112:127]\n\t.endif\n\t.endif\n\t"
#define P1_TS(B)
#define P1_TSOP , "+{v9}"(tsv)
#define P1_TSCL , "s98", "s99"
#define P1_SQ(J) ".if s>0\n\t.set xr, 128+16*p+" #J "\n\tv_and_b32 v4, v8, v[xr]\n\tv_lshlrev_b32 v5, 16, v[xr]\n\tv_fmac_f32 v45, v4, v4\n\tv_fmac_f32 v46, v5, v5\n\t.endif\n\t"
#define P1_HEAD_TEXT \
  "s_setprio 3\n\t.set t, 0\n\t" P1_D0 P1_D1 P1_D2 P1_D3 "s_setprio 2\n\t.set t, 1\n\t" P1_D0 P1_D1 P1_D2 P1_D3 "s_setprio 0"
#define P1_TEXT \
  "v_mov_b32 v8, 0xffff0000\n\t.set s, 0\n\t.rept 9\n\t.set p, s-1\n\t.set t, s+3\n\t" P1_TS("44") \
  ".if s==0\n\ts_waitcnt vmcnt(6) lgkmcnt(0)\n\t.elseif s==1\n\ts_waitcnt vmcnt(10) lgkmcnt(0)\n\t.elseif s<6\n\ts_waitcnt vmcnt(7) lgkmcnt(0)\n\t.elseif s==6\n\ts_waitcnt vmcnt(5) lgkmcnt(0)\n\t" \
  ".elseif s==7\n\ts_waitcnt vmcnt(4) lgkmcnt(0)\n\t.else\n\ts_waitcnt lgkmcnt(0)\n\t.endif\n\t.if s<8\n\ts_barrier\n\t.endif\n\t" P1_TS("54") \
  ".if s==0\n\t" P1_RD(0) P1_RD(1) P1_RD(2) P1_RD(3) P1_RQ(0) P1_RQ(1) ".set t, 2\n\t" P1_D0 P1_D1 P1_D2 P1_D3 ".set t, 3\n\t" P1_D0 P1_D1 P1_D2 P1_D3 ".else\n\t" \
  P1_MM(1) P1_D0 P1_RD(1) P1_SQ(0) P1_MM(2) P1_D1 P1_RD(2) P1_SQ(1) \
  ".if (s&1)==0\n\tv_smfmac_f32_16x16x64_bf16 v[12:15], v[40:43], v[64:71], v44\n\tv_smfmac_f32_16x16x64_bf16 v[16:19], v[40:43], v[48:55], v44\n\t.endif\n\t" \
  P1_MM(3) P1_D2 P1_RD(3) P1_SQ(2) P1_MM(0) P1_D3 P1_RD(0) P1_SQ(3) P1_MQ(0) P1_RQ(0) P1_RQ(1) P1_SQ(4) P1_SQ(5) P1_MQ(1) P1_SQ(6) \
  "v_smfmac_f32_16x16x64_bf16 v[20:23], v[40:43], v[128+16*p:135+16*p], v44\n\t" P1_SQ(7) \
  ".endif\n\t.set s, s+1\n\t.endr\n\ts_nop 7"
#define TL_W(N) ".if (" N "&3)==0\n\t.set w, 56+((" N "/4)&3)*4\n\t.else\n\t.set w, 72+(((" N "/4)*3+(" N "&3)-1)%%6)*4\n\t.endif\n\t"
#define TL_RD ".set n, f+5\n\t.if n<64\n\t.set o, ((n/4)&7)*8192\n\t.set b, 24+(n/32)*4+(n&3)\n\t" TL_W("n") \
              "ds_read_b64_tr_b16 v[w:w+1], v[b] offset:o\n\tds_read_b64_tr_b16 v[w+2:w+3], v[b] offset:o+512\n\t.endif\n\t"
#define TL_MM(A, C) ".if f<59\n\ts_waitcnt lgkmcnt(10)\n\t.else\n\ts_waitcnt lgkmcnt(2*(63-f))\n\t.endif\n\t" TL_W("f") ".set x, 128+(f&31)*4\n\t" \
              ".if (f&31)==0\n\tv_mfma_f32_32x32x16_bf16 v[" A "], v[w:w+3], v[x:x+3], 0\n\t.else\n\tv_mfma_f32_32x32x16_bf16 v[" A "], v[w:w+3], v[x:x+3], v[" A "]\n\t.endif\n\t" \
              ".if (f&7)==4\n\tv_smfmac_f32_16x16x64_bf16 v[" C "], v[40:43], v[w-4:w+3], v44\n\t.endif\n\t"
#define TILE_SUB_TEXT \
  ".Ltile_sub:\n\t.set f, -5\n\t.rept 5\n\t" TL_RD ".set f, f+1\n\t.endr\n\t" \
  ".set k, 16\n\t.rept 8\n\tv_mov_b32 v[k], 0\n\t.set k, k+1\n\t.endr\n\t" \
  ".rept 32\n\t" TL_RD TL_MM("96:111", "16:19") ".set f, f+1\n\t.endr\n\t" \
  "s_cmp_eq_u32 s46, 0\n\ts_cbranch_scc1 .Ltile_ret\n\ts_cmp_eq_u32 s46, 1\n\ts_cbranch_scc1 .Ltile_two\n\t" \
  "v_mov_b32 v45, %c0\n\tv_mov_b32 v46, 1\n\ts_waitcnt vmcnt(0)\n\ts_mov_b64 exec, 1\n\tds_add_u32 v45, v46\n\ts_mov_b64 exec, -1\n\t" \
  ".Ltile_two:\n\t.rept 32\n\t" TL_RD TL_MM("112:127", "20:23") ".set f, f+1\n\t.endr\n\t" \
  ".Ltile_ret:\n\ts_waitcnt lgkmcnt(0)\n\ts_nop 9\n\ts_setpc_b64 s[44:45]\n\t"
#define TILES(BASES, MODE)   \
  asm volatile("s_cmp_eq_u32 s46, 3\n\ts_cbranch_scc1 1f\n\ts_call_b64 s[44:45], .Ltile_sub\n\t1:" : "+{v[96:111]}"(accA), "+{v[112:127]}"(accB), "={v[16:23]}"(cst) \
               : "{v[128:159]}"(X0), "{v[160:191]}"(X1), "{v[192:223]}"(X2), "{v[224:255]}"(X3), "{v[24:31]}"(BASES), "{v[40:43]}"(sel), "{v44}"(selidx), \
                 "{s46}"(MODE) : "memory", "scc", "s44", "s45", "v45", "v46", CLOB_W)

#define E(r) "v_exp_f32 v" #r ", v" #r "\n\t"
#define QK(Dst, Q) "v_mfma_f32_32x32x16_bf16 v[" Dst "], v[144:147], v[" Q "], v[112:127]\n\t"
#define PV(O, V, P) "v_smfmac_f32_32x32x32_bf16 v[" O "], v[" V "], v[" P "], v164\n\t"
#define UA(OP, VP1, QN, O, V0, X0, X1) E(16) E(17) PV(OP, VP1, "40:47") E(18) E(19) E(20) X0 QK("32:47", QN) X1 E(21) E(22) E(23) E(24) E(25) E(26) PV(O, V0, "16:23") E(27) E(28) E(29) E(30) E(31)
#define UB(OP, VP1, QN, O, V0, X0, X1) E(32) E(33) PV(OP, VP1, "24:31") E(34) E(35) E(36) X0 QK("16:31", QN) X1 E(37) E(38) E(39) E(40) E(41) E(42) PV(O, V0, "32:39") E(43) E(44) E(45) E(46) E(47)
#define TR(D, OFF) "ds_read_b64_tr_b16 v[" D "], v166 offset:" OFF "\n\t"
#define KRD(OFF) "ds_read_b128 v[144:147], v165 offset:" OFF "\n\t"
#define WT "s_waitcnt lgkmcnt(0)\n\t"
#define VADV "v_add_u32 v166, v167, v166\n\t"
#define TILE4(VP1, V0, V1, N0, N1, N2, N3, OK)                                          \
  UA("96:111", VP1, "132:135", "48:63", V0, VADV, TR(N0, "0") TR(N1, "128"))            \
  UB("48:63", V1, "136:139", "64:79", V0, "", TR(N2, "512") TR(N3, "640"))              \
  UA("64:79", V1, "140:143", "80:95", V0, "", KRD(OK))                                  \
  UB("80:95", V1, "128:131", "96:111", V0, WT, "")

#if defined(__HIP_DEVICE_COMPILE__)
#define NOPK __attribute__((target("no-packed-fp32-ops")))
#else
#define NOPK
#endif
#define LAM NOPK __attribute__((always_inline))
__global__ NOPK __launch_bounds__(512, 2) void fused_kernel(const char* __restrict__ x, const char* __restrict__ wqkv, const char* __restrict__ wo,
                                                       const char* __restrict__ w1, const char* __restrict__ b1, const char* __restrict__ w2,
                                                       const char* __restrict__ b2, char* __restrict__ out, char* __restrict__ ws) {
  extern __shared__ __attribute__((aligned(16))) char smem[];
  const int wid = __builtin_amdgcn_readfirstlane(threadIdx.x >> 6);
  const int xcd = blockIdx.x & 7, idx = blockIdx.x >> 3;
  const int g = wid >> 2, tg = wid & 3;
  int lane, r, hh, ch, q4, p4, p4s;
  unsigned l16;
#define RELANE() do { asm volatile("v_mbcnt_lo_u32_b32 %0, -1, 0\n\tv_mbcnt_hi_u32_b32 %0, -1, %0" : "=v"(lane)); __builtin_assume(lane >= 0 && lane < 64); \
    r = lane & 31; hh = lane >> 5; ch = (lane >> 4) & 1; q4 = (lane & 15) >> 2; p4 = lane & 3; l16 = lane * 16; \
    p4s = ((p4 & 1) << 1) | (p4 >> 1);   } while (0)
  RELANE();
  const int rs = (r & 19) | ((r & 4) << 1) | ((r & 8) >> 1);
  unsigned tsv = 0;
  TS(0);
  const int mt = (xcd >> 1) * 4 + (idx & 3), np = (xcd & 1) * 8 + (idx >> 2);
  const bool isk = np < 8, even = !(np & 1);
  const int f01 = even ? 2 * np : 32 + (np & ~1), f2 = even ? 2 * np + 2 : 48 + (np >> 1);
  const unsigned vp = ((lane & 7) ^ ((lane >> 2) & 4)) << 4, vf = (lane >> 3) * 3584 + vp;
  u64 sx = (u64)(x + (size_t)(mt * 128 + 16 * wid) * 1024), sw = (u64)(wqkv + 1024 + np * 128 + (size_t)(8 * wid) * 3072),
      sq = (u64)(wqkv + np * 64 + (size_t)(16 * wid) * 3072), sf = (u64)(w1 + f01 * 64 + (size_t)(16 * wid) * 3584);
  const unsigned vx0 = (lane >> 3) * 1024 + (((lane & 7) ^ (lane >> 4)) << 4);
  const u32x2 vx = {vx0, (vx0 ^ 64) + 8192}, vwq = {(lane >> 3) * 3072 + vp, (lane >> 2) * 3072u + (lane & 3) * 16}, vff = {vf, vf + 8 * 3584};
  asm volatile(P1_HEAD_TEXT : "+{s[48:49]}"(sx), "+{s[50:51]}"(sw), "+{s[52:53]}"(sq)
               : "{v[34:35]}"(vx), "{v[36:37]}"(vwq), "{s56}"(wid * 1024), "{s57}"(wid * 2048) : "memory", "scc");
  unsigned bias0, bias1, pf_junk;
  {
    const unsigned bo = ((g ? f2 : f01) * 32 + rs) * 2, bo1 = (g && !even) ? bo : bo + 64;
    u64 pc;
    unsigned len;
    asm volatile("s_getpc_b64 %0\n\t.Lpf_base:\n\ts_mov_b32 %1, .Ltile_ret-.Lpf_base" : "=s"(pc), "=s"(len));
    const u64 p01 = (tg & 1) ? (u64)wqkv : (u64)x, p23 = (tg & 1) ? pc : (u64)w1, pb = (tg & 2) ? p23 : p01;
    const unsigned n01 = (tg & 1) ? 6144u : 8192u, n23 = (tg & 1) ? 8192u : 7168u, nb = (tg & 2) ? n23 : n01;
    const unsigned o0 = (tg == 3 ? (idx & 3) : blockIdx.x) * nb, lim = tg == 3 ? len : o0 + nb - 128;
    const unsigned co = o0 + (g ? lane * 128 : 0);
    asm volatile("global_load_ubyte %2, %5, %6\n\tglobal_load_ushort %0, %3, %7\n\tglobal_load_ushort %1, %4, %7"
                 : "=&{v10}"(bias0), "=&{v11}"(bias1), "=&{v47}"(pf_junk) : "v"(bo), "v"(bo1), "v"(co < lim ? co : lim), "s"(pb), "s"(b1) : "memory");
  }
  u64 epoch;
  asm volatile("s_load_dwordx2 %0, %1, 0x0" : "=s"(epoch) : "s"(ws) : "memory");
  if (wid == 4) {
    lds_wu(L_CNT + r * 4, 0u);
    lds_w128(L_CONST + (lane < 48 ? lane : 47) * 16, (u32x4){0x3f80u, 0u, 0u, 0u});
  }
  const unsigned ones = 0x3f803f80u;
  const bool selon = (lane & 3) < 2, selhi = (lane >> 2) & 1;
  const u32x4 sel = {selon && !selhi ? ones : 0u, selon && !selhi ? ones : 0u, selon && selhi ? ones : 0u, selon && selhi ? ones : 0u};
  const int selidx = (lane & 3) == 0 ? 0x4444 : 0xEEEE;
  const unsigned wl = (8 * hh + q4) * 64 + ch * 32 + p4s * 8;
  const unsigned wp = (8 * hh + q4) * 128 + (q4 >> 1) * 64 + ch * 32 + p4s * 8;
  const int rot = tg ^ (2 * g);
  u32x4 wa;
  u32x32 X0, X1, X2, X3;
  f32x16 accA, accB;
  auto put_cs = [&](float c, int aug, unsigned bias) LAM {
    const float chi = rbf(c);
    const unsigned cw = pin(pk_bf16(chi, c - chi)), bw = pin(tg == 0 ? bias : 0u);
    lds_wu(L_AUG + aug * 1024 + l16 + tg * 4, hh ? bw : cw);
  };
  {
    f32x4 cs = {0.f, 0.f, 0.f, 0.f}, cq = cs, xs = cs;
    float xq0 = 0.f, xq1 = 0.f;
    const int row = 32 * tg + r;
    u32x4 xa;
#pragma unroll
    for (int i = 0; i < 4; ++i) {
      xa[i] = row * 128 + (((2 * (i ^ rot) + hh) ^ ((row >> 1) & 7)) << 4);
      wa[i] = pin(((wp + (i ^ rot) * 2048) ^ (g * 64)) + 4 * U);
    }
    const u32x2 qa = {(wl ^ (g * 32)) + rot * 1024 + 6 * U, (wl ^ (g * 32)) + (rot ^ 1) * 1024 + 6 * U};
    TS(1);
    asm volatile(P1_TEXT
                 : "={v[128:159]}"(X0), "={v[160:191]}"(X1), "={v[192:223]}"(X2), "={v[224:255]}"(X3), "={v[96:111]}"(accA), "={v[112:127]}"(accB),
                   "+{v[12:15]}"(cs), "+{v[16:19]}"(cq), "+{v[20:23]}"(xs), "+{v45}"(xq0), "+{v46}"(xq1), "+{s[48:49]}"(sx), "+{s[50:51]}"(sw), "+{s[52:53]}"(sq),
                   "+{s[54:55]}"(sf), "+s"(epoch), "+{v10}"(bias0), "+{v11}"(bias1), "+{v47}"(pf_junk) P1_TSOP
                 : "{v[24:27]}"(xa), "{v[28:31]}"(wa), "{v[32:33]}"(qa), "{v[34:35]}"(vx), "{v[36:37]}"(vwq), "{v[38:39]}"(vff), "{s56}"(wid * 1024),
                   "{s57}"(wid * 2048), "{v[40:43]}"(sel), "{v44}"(selidx)
                 : "memory", "scc", "v4", "v5", "v8", "v48", "v49", "v50", "v51", "v52", "v53", "v54", "v55", CLOB_W P1_TSCL);
    TS(2);
    RELANE();
    lds_wf4(8 * U + wid * 2048 + l16, (f32x4){accB[8], accB[9], accB[10], accB[11]});
    lds_wf4(8 * U + wid * 2048 + 1024 + l16, (f32x4){accB[12], accB[13], accB[14], accB[15]});
    lds_wf2(L_STAT + (32 * tg + r) * 16 + g * 8, (f32x2){xs[0] + xs[1], swap_add(xq0 + xq1)});
    put_cs(cs[0] + cs[1], g, 0u);
    put_cs(cq[0] + cq[1], 2 + g, 0u);
  }
  BARRIER();
  TS(3);
  float rstd;
  u32x4 xaug;
  {
    const f32x4 st = lds_rf4(L_STAT + (32 * tg + r) * 16);
    const float mean = (st[0] + st[2]) * (1.f / 512.f);
    const float var = fmaxf((st[1] + st[3]) * (1.f / 512.f) - mean * mean, 0.f);
    const float sd = rbf(__builtin_amdgcn_sqrtf(rbf(rbf(var) + 1e-5f)));
    rstd = __builtin_amdgcn_rcpf(sd);
    const unsigned nm = pin(pk_bf16(-mean, -mean)), sw_ = pin(pk_bf16(sd, 0.f)), nz = hh ? 0u : nm;
    xaug = (u32x4){hh ? sw_ : nm, nz, nz, nz};
  }
  glds((const void*)sf, vff[0], 3 * U + wid * 2048);
  glds((const void*)sf, vff[1], 3 * U + wid * 2048 + 1024);
  const int tt = mt * 4 + tg;
  auto finish = [&](f32x16 a, int aug, bool relu2, u32x4& o0, u32x4& o1) LAM {
    a = mfma32(lds_r128(L_AUG + aug * 1024 + l16), xaug, a);
#pragma unroll
    for (int i = 0; i < 8; ++i) {
      float u = a[2 * i] * rstd, v = a[2 * i + 1] * rstd;
      if (relu2) { u = fmaxf(u, 0.f); v = fmaxf(v, 0.f); u *= u; v *= v; }
      if (i < 4) o0[i] = pk_bf16(u, v); else o1[i - 4] = pk_bf16(u, v);
    }
  };
  {
    u32x4 o0, o1, oq;
    const f32x16 a = mfma32(lds_r128(L_AUG + (2 + g) * 1024 + l16), xaug, accB);
    const f32x4 r0 = lds_rf4(8 * U + (wid ^ 4) * 2048 + l16), r1 = lds_rf4(8 * U + (wid ^ 4) * 2048 + 1024 + l16);
    finish(accA, g, false, o0, o1);
    const int head = (np & 7) * 4 + g * 2;
    char* kv = ws + WS_KV + ((size_t)head * 64 + tt) * 2048;
    const unsigned vo = isk ? l16 : 1024 + r * 32 + hh * 16;
    st16_dev(kv, vo, o0);
    st16_dev(kv + 131072, vo, o1);
    const float fq = rstd * (0.25f * 1.4426950408889634f);
    oq[0] = pk_bf16((a[0] + r0[0]) * fq, (a[1] + r0[1]) * fq);
    oq[1] = pk_bf16((a[2] + r0[2]) * fq, (a[3] + r0[3]) * fq);
    oq[2] = pk_bf16((a[4] + r1[0]) * fq, (a[5] + r1[1]) * fq);
    oq[3] = pk_bf16((a[6] + r1[2]) * fq, (a[7] + r1[3]) * fq);
    st16_dev(ws + WS_Q + ((size_t)(2 * np + g) * 64 + tt) * 1024, l16, oq);
    if (isk) {
      const float n0 = swap_add(sq8(o0)), n1 = swap_add(sq8(o1));
      const unsigned m = umax32(f2u(hh ? n1 : n0));
      st4_dev2(ws + WS_KMAX + (head * 64 + tt) * 4, hh * 256, m);
    }
  }
  const u64 tag = [&]() LAM { u64 z = epoch + 0x9E3779B97F4A7C15ull; z = (z ^ (z >> 30)) * 0xBF58476D1CE4E5B9ull; z = (z ^ (z >> 27)) * 0x94D049BB133111EBull; return z ^ (z >> 31); }();
  TS(4);
  if (isk) WAITV(4); else WAITV(3);
  BARRIER();
  TS(5);
  u32x4 hp[4];
  const int head = xcd * 4 + (idx >> 3), qb = idx & 7;
  const int m2 = xcd * 4 + (idx & 3), n2 = idx >> 2;
  char* hb = ws + WS_H + (size_t)tt * 112 * 1024;
  const int fr0 = (blockIdx.x & 31) * 9 + tg * 3;
  const char* kv = ws + WS_KV + (size_t)head * 131072;
  f32x8 cst;
  {
    const unsigned x0 = g ? (even ? 64 : ((f2 & 1) ^ 1) * 64) : 0, x1 = g ? 0 : 64, d = g ? U : 0u - 4 * U;
    const u32x8 bases = {(wa[0] ^ x0) + d, (wa[1] ^ x0) + d, (wa[2] ^ x0) + d, (wa[3] ^ x0) + d, (wa[0] ^ x1) + d, (wa[1] ^ x1) + d, (wa[2] ^ x1) + d, (wa[3] ^ x1) + d};
    TILES(bases, g ? 3 : 2);
    if (!g) {
      put_cs(cst[0] + cst[1], 0, bias0);
      put_cs(cst[4] + cst[5], 1, bias1);
    } else {
      const char* wq = w1 + (f2 & ~1) * 64 + (size_t)(16 * tg) * 3584;
#pragma unroll 1
      for (int i = 0; i < 8; ++i) {
        glds(wq + (size_t)i * 64 * 3584, vff[0], 5 * U + i * 8192 + tg * 2048);
        glds(wq + (size_t)i * 64 * 3584, vff[1], 5 * U + i * 8192 + tg * 2048 + 1024);
      }
      TS(6);
      WAITV(16);
      cnt_add<C_DRAIN>();
      TS(7);
      if (tg == 0) {
        cnt_wait<C_DRAIN, 8>();
        st8_dev1(ws + WS_FA, (np * 16 + mt) * 8, tag);
        WAITV(1);
      } else {
        WAITV(0);
      }
    }
    TS(8);
    BARRIER();
    TS(9);
    if (!g) __builtin_amdgcn_s_setprio(2);
    TILES(bases, g ? (even ? 1 : 0) : 3);
  }
  RELANE();
  const int qg = tg & 1, kh = tg >> 1;
  u32x4 qf[4];
  unsigned kmu;
  if (g) {
    TS(10);
    __builtin_amdgcn_s_setprio(3);
    put_cs(cst[0] + cst[1], 2, bias0);
    put_cs(cst[4] + cst[5], 3, bias1);
    cnt_add<C_CS>();
    cnt_wait<C_CS, 4>();
    asm volatile("" : "={v[128:131]}"(qf[0]), "={v[132:135]}"(qf[1]), "={v[136:139]}"(qf[2]), "={v[140:143]}"(qf[3]), "={v144}"(kmu));
  } else {
  {
    const unsigned fkv = pin((head >> 2) * 16 + ch * 128 + (lane & 15)), fq = pin((head >> 1) * 16 + 2 * qb + (lane & 1));
    poll_flags<1>(ws, WS_FA + (hh ? fq : fkv) * 8, tag);
  }
  TS(10);
  {
    const char* qp = ws + WS_Q + ((size_t)head * 64 + qb * 8 + 4 * qg) * 1024;
    asm volatile("global_load_dwordx4 %0, %5, %7 sc1\n\tglobal_load_dwordx4 %1, %5, %7 offset:1024 sc1\n\tglobal_load_dwordx4 %2, %6, %7 sc1\n\t"
                 "global_load_dwordx4 %3, %6, %7 offset:1024 sc1\n\tglobal_load_dword %4, %8, %9 sc1"
                 : "=&{v[128:131]}"(qf[0]), "=&{v[132:135]}"(qf[1]), "=&{v[136:139]}"(qf[2]), "=&{v[140:143]}"(qf[3]), "=&{v144}"(kmu)
                 : "v"(l16 + kh * 2048), "v"(l16 + (1 - kh) * 2048), "s"(qp), "v"(lane * 4), "s"(ws + WS_KMAX + head * 256) : "memory");
    glds4_dev(kv + kh * 4 * U + qg * 4096, l16, kh * 4 * U + qg * 4096);
    glds4_dev(kv + kh * 4 * U + 8192 + qg * 4096, l16, kh * 4 * U + 8192 + qg * 4096);
  }
  }
  finish(accA, 2 * g, true, hp[0], hp[1]);
  finish(accB, 2 * g + 1, true, hp[2], hp[3]);
  asm volatile("" : "+v"(hp[0]), "+v"(hp[1]), "+v"(hp[2]), "+v"(hp[3]));
  TS(11);
  RELANE();
  if (g) {
    cnt_wait<C_KV1, 4>();
    TS(12);
#pragma unroll
    for (int i = 1; i < 4; ++i) { glds4_dev(kv + i * U + tg * 4096, l16, i * U + tg * 4096); glds4_dev(kv + (4 + i) * U + tg * 4096, l16, (4 + i) * U + tg * 4096); }
    if (tg < 3) {
#pragma unroll
      for (int i = 0; i < 3; ++i) {
        const int band = (fr0 + i) >> 1, nt = (blockIdx.x >> 5) * 2 + ((fr0 + i) & 1);
        glds((band < 112 ? w2 + (size_t)band * 16384 : wo + (size_t)(band - 112) * 16384) + nt * 64, (lane >> 2) * 1024 + (lane & 3) * 16, 8 * U + tg * 4096 + i * 1024);
      }
      WAITV(3);
    } else {
      WAITV(0);
    }
    cnt_add<C_KV2>();
    TS(13);
#pragma unroll
    for (int i = 0; i < 4; ++i) st16_dev(hb + (size_t)(f2 * 2 + ((even || i < 2) ? i : i - 2)) * 1024, l16, (even || i < 2) ? hp[i] : hp[i - 2]);
    if (tg < 3) {
      WAITV(4);
#pragma unroll
      for (int i = 0; i < 3; ++i) {
        const int band = (fr0 + i) >> 1, nt = (blockIdx.x >> 5) * 2 + ((fr0 + i) & 1);
        const u32x4 v = lds_trf(8 * U + tg * 4096 + i * 1024 + (8 * hh + q4) * 64 + ch * 32 + p4s * 8, 256);
        st16_dev(ws + (band < 112 ? WS_W2 + (nt * 112 + band) * 1024 : WS_WO + (nt * 32 + band - 112) * 1024), l16, v);
      }
    }
    WAITV(0);
    cnt_add<C_HDRAIN>();
    TS(14);
    if (tg == 0) {
      cnt_wait<C_HDRAIN, 8, 8>();
      st8_dev1(ws + WS_FB, blockIdx.x * 8, tag);
      TS(15);
      const unsigned fh = pin(((m2 >> 3) * 2 + ((lane >> 3) & 1)) + 8 * (((m2 >> 1) & 3) + 4 * (lane & 7))), fw = pin(32 * n2 + r);
      poll_flags<8>(ws, WS_FB + (hh ? fw : fh) * 8, tag);
      cnt_add<C_FB>();
    } else {
      cnt_wait<C_FB, 1, 8>();
    }
    TS(16);
    const auto rsrc = __builtin_amdgcn_make_buffer_rsrc(ws, 0, WS_END, 0x00020000);
    const unsigned ho[2] = {WS_H + ((m2 * 2 + (tg >> 1)) * 112 + tg * 28) * 1024, WS_H + ((m2 * 2 + 1 - (tg >> 1)) * 112 + tg * 28) * 1024};
    const unsigned wo2[2] = {WS_W2 + ((n2 * 2 + (tg & 1)) * 112 + tg * 28) * 1024, WS_W2 + ((n2 * 2 + 1 - (tg & 1)) * 112 + tg * 28) * 1024};
    constexpr int DEP = 9;
    u32x4 hf[DEP][2], wf[DEP][2];
    auto ld = [&](int s) LAM {
#pragma unroll
      for (int u = 0; u < 2; ++u) {
        hf[s % DEP][u] = __builtin_amdgcn_raw_buffer_load_b128(rsrc, l16 + (s & 3) * 1024, ho[u] + (s & ~3) * 1024, 16);
        wf[s % DEP][u] = __builtin_amdgcn_raw_buffer_load_b128(rsrc, l16 + (s & 3) * 1024, wo2[u] + (s & ~3) * 1024, 16);
      }
    };
    {
      constexpr int S0 = NBG + DEP, NL = (28 - S0) * 8;
      unsigned junk[8];
#pragma unroll
      for (int i = 0; i < 8; ++i) {
        const unsigned li = (i & 1) * 64 + lane;
        junk[i] = __builtin_amdgcn_raw_buffer_load_b32(rsrc, (li < NL ? li : 0) * 128, ((i >> 1) < 2 ? ho[(i >> 1) & 1] : wo2[(i >> 1) & 1]) + S0 * 1024, 16);
      }
#pragma unroll
      for (int i = 0; i < 8; ++i) asm volatile("" ::"v"(junk[i]));
    }
    cnt_wait<C_PROG, 4, 16>();
#pragma unroll
    for (int i = 0; i < 4; ++i)
      glds4_dev(ws + WS_WO + ((n2 * 2 + (i >> 1)) * 32 + tg * 8 + (i & 1) * 4) * 1024, l16, (i >> 1) * 4 * U + (tg * 8 + (i & 1) * 4) * 1024);
    WAITV(0);
    cnt_add<C_WO>();
    TS(17);
    f32x16 fa[2][2];
#pragma unroll
    for (int a = 0; a < 2; ++a)
#pragma unroll
      for (int b = 0; b < 2; ++b)
#pragma unroll
        for (int i = 0; i < 16; ++i) fa[a][b][i] = 0.f;
#pragma unroll
    for (int s = 0; s < DEP; ++s) ld(s);
    SB();
    __builtin_amdgcn_s_setprio(0);
#pragma unroll
    for (int s = 0; s < 28; ++s) {
      if (s == NBG) { TS(30); asm volatile("s_barrier" ::: "memory"); TS(31); }
#pragma unroll
      for (int a = 0; a < 2; ++a)
#pragma unroll
        for (int b = 0; b < 2; ++b) fa[a][b] = mfma32(wf[s % DEP][b], hf[s % DEP][a], fa[a][b]);
      SB();
      if (s + DEP < 28) ld(s + DEP);
      SB();
    }
    TS(32);
    RELANE();
    const int n0 = n2 * 64 + (tg & 1) * 32 + 8 * hh;
    const u32x4 bb0 = *(const u32x4*)(b2 + n0 * 2), bb1 = *(const u32x4*)(b2 + n0 * 2 + 32);
    auto fslot = [&](int dest, int d) LAM { const int k = dest * 3 + d - 1; return (k < 8 ? 2 * U + k * 4096 : 6 * U + (k - 8) * 4096) + l16; };
#pragma unroll
    for (int d = 1; d < 4; ++d) {
      const f32x16 v = fa[d >> 1][d & 1];
      const unsigned a = fslot(tg ^ d, d);
#pragma unroll
      for (int j = 0; j < 4; ++j) lds_wf4(a + j * 1024, (f32x4){v[4 * j], v[4 * j + 1], v[4 * j + 2], v[4 * j + 3]});
    }
    cnt_add<C_FX>();
    TS(33);
    cnt_wait<C_FX, 4>();
    TS(34);
    f32x16 z = fa[0][0];
#pragma unroll
    for (int d = 1; d < 4; ++d)
#pragma unroll
      for (int j = 0; j < 4; ++j) {
        const f32x4 v = lds_rf4(fslot(tg, d) + j * 1024);
#pragma unroll
        for (int i = 0; i < 4; ++i) z[4 * j + i] += v[i];
      }
#pragma unroll
    for (int i = 0; i < 16; ++i) {
      const unsigned bw = i < 8 ? bb0[i >> 1] : bb1[(i - 8) >> 1];
      const float y = z[i] + u2f((i & 1) ? (bw & 0xffff0000u) : (bw << 16));
      z[i] = y * __builtin_amdgcn_rcpf(1.f + __builtin_amdgcn_exp2f(y * -1.4426950408889634f));
    }
#pragma unroll
    for (int j = 0; j < 4; ++j) lds_wf4(7 * U + tg * 4096 + j * 1024 + l16, (f32x4){z[4 * j], z[4 * j + 1], z[4 * j + 2], z[4 * j + 3]});
    cnt_add<C_G>();
    TS(35);
    TSFLUSH();
    return;
  }
  RELANE();
  {
    asm volatile("s_waitcnt vmcnt(8)" : "+{v[128:131]}"(qf[0]), "+{v[132:135]}"(qf[1]), "+{v[136:139]}"(qf[2]), "+{v[140:143]}"(qf[3]), "+{v144}"(kmu)::"memory");
  }
  TS(13);
  kmu = umax32(kmu);
  { auto t2 = __builtin_amdgcn_permlane32_swap(kmu, kmu, false, false); kmu = umax(t2[0], t2[1]); }
  const unsigned qn = umax(umax(f2u(swap_add(sq8(qf[0]))), f2u(swap_add(sq8(qf[1])))),
                           umax(f2u(swap_add(sq8(qf[2]))), f2u(swap_add(sq8(qf[3])))));
  const float bound = __builtin_amdgcn_sqrtf(u2f(qn) * u2f(kmu)) * 1.001f;
  f32x16 o[4];
  float mx[4];
  unsigned ka = kh * 4 * U + lane * 16;
  const unsigned vac = pin(L_CONST + (p4 >> 1) * 8), vad = pin(kh * 4 * U + 1024 + (8 * hh + q4) * 32 + p4s * 8);
  unsigned va = ch ? vac : vad;
  const unsigned vstep = ch ? 0u : 2048u;
#pragma unroll
  for (int u = 0; u < 4; ++u)
#pragma unroll
    for (int i = 0; i < 16; ++i) o[u][i] = 0.f;
  asm volatile("" : "+v"(o[0]), "+v"(o[1]), "+v"(o[2]), "+v"(o[3]), "+v"(va));
  WAITV(0);
  cnt_add<C_KV1>();
  cnt_wait<C_KV1, 4>();
  TS(14);
  char* hst = hb + (size_t)f01 * 2048;
  if (__builtin_amdgcn_ballot_w64(bound <= BOUND_LIMIT) == ~0ull) {
    f32x16 cneg;
#pragma unroll
    for (int i = 0; i < 16; ++i) cneg[i] = -bound;
    const u32x8 q01 = __builtin_shufflevector(qf[0], qf[1], 0, 1, 2, 3, 4, 5, 6, 7), q23 = __builtin_shufflevector(qf[2], qf[3], 0, 1, 2, 3, 4, 5, 6, 7);
    asm volatile(
        "v_mov_b32 v164, 0xdddd\n\t"
        KRD("0") TR("148:149", "0") TR("150:151", "128") TR("152:153", "512") TR("154:155", "640")
        "v_mov_b32 v40, 0\n\tv_mov_b32 v41, 0\n\tv_mov_b32 v42, 0\n\tv_mov_b32 v43, 0\n\t"
        "v_mov_b32 v44, 0\n\tv_mov_b32 v45, 0\n\tv_mov_b32 v46, 0\n\tv_mov_b32 v47, 0\n\t"
        "v_mov_b32 v160, 0\n\tv_mov_b32 v161, 0\n\tv_mov_b32 v162, 0\n\tv_mov_b32 v163, 0\n\t"
        "v_mov_b32 v168, %[cnt]\n\tv_mov_b32 v169, 1\n\t"
        WT
        QK("16:31", "128:131")
        "s_mov_b32 s40, 0\n\t"
        "s_mov_b32 s41, 3\n\t"
        "s_nop 10\n\t"
        "1:\n\t"
        TILE4("160:163", "148:151", "152:155", "156:157", "158:159", "160:161", "162:163", "2048")
        TILE4("152:155", "156:159", "160:163", "148:149", "150:151", "152:153", "154:155", "4096")
        "v_add_u32 v165, 0x1000, v165\n\t"
        "s_add_u32 s40, s40, 1\n\t"
        "s_cmp_lt_u32 s40, s41\n\t"
        "s_cbranch_scc1 1b\n\t"
        "s_cmp_eq_u32 s40, 16\n\t"
        "s_cbranch_scc1 4f\n\t"
        "s_cmp_eq_u32 s40, 3\n\t"
        "s_cbranch_scc0 3f\n\t"
        "s_mov_b32 s41, 0\n\t"
        "2:\n\t"
        "ds_read_b32 v170, v168 offset:%[ckv]\n\t"
        "s_add_u32 s41, s41, 1\n\t"
        "s_waitcnt lgkmcnt(0)\n\t"
        "v_readfirstlane_b32 s42, v170\n\t"
        "s_bitcmp1_b32 s41, 22\n\t"
        "s_cselect_b32 s42, 4, s42\n\t"
        "s_cmp_lt_u32 s42, 4\n\t"
        "s_cbranch_scc1 2b\n\t"
        "global_store_dwordx4 %[l16], v[172:175], %[hst] sc1\n\t"
        "global_store_dwordx4 %[l16], v[176:179], %[hst] offset:1024 sc1\n\t"
        "global_store_dwordx4 %[l16], v[180:183], %[hst] offset:2048 sc1\n\t"
        "global_store_dwordx4 %[l16], v[184:187], %[hst] offset:3072 sc1\n\t"
        "s_mov_b32 s41, 5\n\t"
        "s_branch 1b\n\t"
        "3:\n\t"
        "s_waitcnt vmcnt(0)\n\t"
        "s_mov_b64 exec, 1\n\t"
        "ds_add_u32 v168, v169 offset:%[chd]\n\t"
        "v_add_u32 v168, 4, v168\n\t"
        "s_mov_b64 exec, -1\n\t"
        "s_cmp_eq_u32 s40, 5\n\t"
        "s_cselect_b32 s41, 8, 16\n\t"
        "s_branch 1b\n\t"
        "4:\n\t"
        PV("96:111", "160:163", "40:47")
        "s_nop 15\n\t"
        : "+{v[48:63]}"(o[0]), "+{v[64:79]}"(o[1]), "+{v[80:95]}"(o[2]), "+{v[96:111]}"(o[3]), "+{v165}"(ka), "+{v166}"(va)
        : "{v[112:127]}"(cneg), "{v[128:135]}"(q01), "{v[136:143]}"(q23), "{v167}"(vstep), [cnt] "i"(L_CNT), [chd] "i"(4 * C_HDRAIN),
          [ckv] "i"(4 * C_KV2), "{v[172:175]}"(hp[0]), "{v[176:179]}"(hp[1]), "{v[180:183]}"(hp[2]), "{v[184:187]}"(hp[3]), [l16] "v"(l16), [hst] "s"(hst)
        : "memory", "scc", "s40", "s41", "s42", "v16", "v17", "v18", "v19", "v20", "v21", "v22", "v23", "v24", "v25", "v26", "v27", "v28", "v29", "v30",
          "v31", "v32", "v33", "v34", "v35", "v36", "v37", "v38", "v39", "v40", "v41", "v42", "v43", "v44", "v45", "v46", "v47", "v144", "v145",
          "v146", "v147", "v148", "v149", "v150", "v151", "v152", "v153", "v154", "v155", "v156", "v157", "v158", "v159", "v160", "v161", "v162",
          "v163", "v164", "v168", "v169", "v170");
#pragma unroll
    for (int u = 0; u < 4; ++u) mx[u] = bound;
  } else {
#pragma unroll
    for (int i = 0; i < 4; ++i) st16_dev(hst + i * 1024, l16, hp[i]);
    WAITV(0);
    cnt_add<C_HDRAIN>();
    cnt_wait<C_KV2, 4>();
    f32x16 z;
#pragma unroll
    for (int i = 0; i < 16; ++i) z[i] = 0.f;
#pragma unroll
    for (int u = 0; u < 4; ++u) mx[u] = -3.0e38f;
#pragma unroll 1
    for (int t = 0; t < 32; ++t) {
      if (t == 16) cnt_add<C_PROG>();
      const u32x4 kf = lds_r128(ka), v0 = lds_trf(va, 128), v1 = lds_trf(va + 512, 128);
      ka += 2048; va += vstep;
#pragma unroll
      for (int u = 0; u < 4; ++u) {
        f32x16 s = mfma32(kf, qf[u], z);
        float m = s[0];
#pragma unroll
        for (int i = 1; i < 16; ++i) m = fmaxf(m, s[i]);
        m = fmaxf(swap_max(m), mx[u]);
        const float al = __builtin_amdgcn_exp2f(mx[u] - m);
        mx[u] = m;
#pragma unroll
        for (int i = 0; i < 16; ++i) { o[u][i] *= al; s[i] = __builtin_amdgcn_exp2f(s[i] - m); }
        const f32x8 plo = __builtin_shufflevector(s, s, 0, 1, 2, 3, 4, 5, 6, 7), phi = __builtin_shufflevector(s, s, 8, 9, 10, 11, 12, 13, 14, 15);
        o[u] = __builtin_amdgcn_smfmac_f32_32x32x32_bf16(__builtin_bit_cast(bf16x8, v0), __builtin_bit_cast(bf16x16, plo), o[u], 0xdddd, 0, 0);
        o[u] = __builtin_amdgcn_smfmac_f32_32x32x32_bf16(__builtin_bit_cast(bf16x8, v1), __builtin_bit_cast(bf16x16, phi), o[u], 0xdddd, 0, 0);
      }
    }
  }
  TS(30);
  RELANE();
  cnt_wait<C_WO, 4>();
#pragma unroll
  for (int u = 0; u < 2; ++u) {
    const f32x16 v = o[2 + u];
    lds_wf4(8 * U + tg * 4096 + u * 2048 + l16, (f32x4){v[0], v[1], v[2], v[3]});
    lds_wf4(8 * U + tg * 4096 + u * 2048 + 1024 + l16, (f32x4){v[4], v[5], v[6], v[7]});
    lds_wf(L_STAT + tg * 512 + u * 256 + lane * 4, hh ? mx[2 + u] : v[8]);
  }
  TS(31);
  BARRIER();
  TS(32);
  const int mo = qb * 4 + 2 * qg + kh;
#pragma unroll
  for (int u = 0; u < 2; ++u) {
    const unsigned pa = 8 * U + (tg ^ 2) * 4096 + u * 2048 + l16, za = L_STAT + (tg ^ 2) * 512 + u * 256 + r * 4;
    const f32x4 a = lds_rf4(pa), b = lds_rf4(pa + 1024);
    const float lp = lds_rf(za), mp = lds_rf(za + 128);
    const float mn = fmaxf(mx[u], mp);
    const float f0 = __builtin_amdgcn_exp2f(mx[u] - mn), f1 = __builtin_amdgcn_exp2f(mp - mn);
    const float inv = __builtin_amdgcn_rcpf(o[u][8] * f0 + lp * f1);
    const float g0 = f0 * inv, g1 = f1 * inv;
    u32x4 w;
    w[0] = pk_bf16(o[u][0] * g0 + a[0] * g1, o[u][1] * g0 + a[1] * g1);
    w[1] = pk_bf16(o[u][2] * g0 + a[2] * g1, o[u][3] * g0 + a[3] * g1);
    w[2] = pk_bf16(o[u][4] * g0 + b[0] * g1, o[u][5] * g0 + b[1] * g1);
    w[3] = pk_bf16(o[u][6] * g0 + b[2] * g1, o[u][7] * g0 + b[3] * g1);
    st16_dev(ws + WS_O + ((size_t)(mo * 2 + u) * 32 + head) * 1024, l16, w);
  }
  TS(33);
  WAITV(0);
  TS(34);
  st8_dev1(ws + WS_FC, (mo * 32 + head) * 8, tag);
  const size_t eo = ((size_t)(m2 * 64 + (tg >> 1) * 32 + r) * 512 + n2 * 64 + (tg & 1) * 32 + 8 * hh) * 2;
  const u32x4 xv0 = *(const u32x4*)(x + eo), xv1 = *(const u32x4*)(x + eo + 32);
  u32x4 wf[8][2];
#pragma unroll
  for (int s = 0; s < 8; ++s)
#pragma unroll
    for (int b = 0; b < 2; ++b) wf[s][b] = lds_r128(((b ^ (tg & 1)) * 4 * U) + (tg * 8 + s) * 1024 + l16);
  TS(35);
  poll_flags(ws, WS_FC + (m2 * 32 + tg * 8 + (lane & 7)) * 8, tag);
  TS(36);
  const auto rsrc = __builtin_amdgcn_make_buffer_rsrc(ws, 0, WS_END, 0x00020000);
  u32x4 of[8][2];
#pragma unroll
  for (int s = 0; s < 8; ++s)
#pragma unroll
    for (int a = 0; a < 2; ++a)
      of[s][a] = __builtin_amdgcn_raw_buffer_load_b128(rsrc, l16 + (s & 3) * 1024, WS_O + ((m2 * 2 + (a ^ (tg >> 1))) * 32 + tg * 8 + (s & 4)) * 1024, 16);
  SB();
  f32x16 sa[2][2];
#pragma unroll
  for (int a = 0; a < 2; ++a)
#pragma unroll
    for (int b = 0; b < 2; ++b)
#pragma unroll
      for (int i = 0; i < 16; ++i) sa[a][b][i] = 0.f;
#pragma unroll
  for (int s = 0; s < 8; ++s)
#pragma unroll
    for (int a = 0; a < 2; ++a)
#pragma unroll
      for (int b = 0; b < 2; ++b) sa[a][b] = mfma32(wf[s][b], of[s][a], sa[a][b]);
  asm volatile("" : "+v"(sa[0][0]), "+v"(sa[0][1]), "+v"(sa[1][0]), "+v"(sa[1][1]));
  TS(37);
  auto aslot = [&](int src, int d) LAM { return (d == 3 ? 4 * U : (d - 1) * 4096) + src * 8192 + l16; };
#pragma unroll
  for (int d = 1; d < 4; ++d) {
    const f32x16 v = sa[d >> 1][d & 1];
#pragma unroll
    for (int j = 0; j < 4; ++j) lds_wf4(aslot(tg, d) + j * 1024, (f32x4){v[4 * j], v[4 * j + 1], v[4 * j + 2], v[4 * j + 3]});
  }
  cnt_add<C_AX>();
  TS(38);
  cnt_wait<C_AX, 4>();
  TS(39);
  f32x16 sv = sa[0][0];
#pragma unroll
  for (int d = 1; d < 4; ++d)
#pragma unroll
    for (int j = 0; j < 4; ++j) {
      const f32x4 v = lds_rf4(aslot(tg ^ d, d) + j * 1024);
#pragma unroll
      for (int i = 0; i < 4; ++i) sv[4 * j + i] += v[i];
    }
  TS(40);
  cnt_wait<C_G, 4>();
  TS(41);
  u32x4 res[2];
#pragma unroll
  for (int j = 0; j < 4; ++j) {
    const f32x4 gv = lds_rf4(7 * U + tg * 4096 + j * 1024 + l16);
#pragma unroll
    for (int i = 0; i < 2; ++i) {
      const unsigned xw = j < 2 ? xv0[2 * j + i] : xv1[2 * (j - 2) + i];
      res[j >> 1][2 * (j & 1) + i] = pk_bf16(u2f(xw << 16) + sv[4 * j + 2 * i] * gv[2 * i],
                                             u2f(xw & 0xffff0000u) + sv[4 * j + 2 * i + 1] * gv[2 * i + 1]);
    }
  }
  *(u32x4*)(out + eo) = res[0];
  *(u32x4*)(out + eo + 32) = res[1];
  if (blockIdx.x == 0 && wid == 0) st8_dev1(ws, 0u, tag);
  TS(42);
  TSFLUSH();
  asm volatile("s_endpgm\n\t" TILE_SUB_TEXT :: "i"(L_CNT + 4 * C_DRAIN) : "memory");
}

extern "C" void kernel_launch(void* const* d_in, const int* in_sizes, int n_in, void* d_out, int out_size, void* d_ws, size_t ws_size,
                              hipStream_t stream) {
  (void)hipFuncSetAttribute((const void*)fused_kernel, hipFuncAttributeMaxDynamicSharedMemorySize, L_TOTAL);
  fused_kernel<<<256, 512, L_TOTAL, stream>>>((const char*)d_in[0], (const char*)d_in[1], (const char*)d_in[2], (const char*)d_in[4],
                                              (const char*)d_in[5], (const char*)d_in[6], (const char*)d_in[7], (char*)d_out, (char*)d_ws);
}
```

```cpp
#include <hip/hip_runtime.h>
#include <hip/hip_bf16.h>

typedef __attribute__((ext_vector_type(8))) __bf16 bf16x8;
typedef __attribute__((ext_vector_type(16))) __bf16 bf16x16;
typedef __attribute__((ext_vector_type(2))) __bf16 bf16x2;
typedef __attribute__((ext_vector_type(16))) float f32x16;
typedef __attribute__((ext_vector_type(8))) float f32x8;
typedef __attribute__((ext_vector_type(4))) float f32x4;
typedef __attribute__((ext_vector_type(2))) float f32x2;
typedef __attribute__((ext_vector_type(4))) unsigned u32x4;
typedef __attribute__((ext_vector_type(8))) unsigned u32x8;
typedef __attribute__((ext_vector_type(32))) unsigned u32x32;
typedef __attribute__((ext_vector_type(2))) unsigned u32x2;
typedef __attribute__((ext_vector_type(4))) short s16x4;
typedef unsigned long long u64;
#define LDS_AS __attribute__((address_space(3)))
#define GLB_AS __attribute__((address_space(1)))
#define DI __device__ __forceinline__

constexpr unsigned WS_FLAG = 0;
constexpr unsigned WS_FA = 1024;
constexpr unsigned WS_FB = 4096;
constexpr unsigned WS_FC = 8192;
constexpr unsigned WS_KMAX = 16384;
constexpr unsigned WS_Q = 32768;
constexpr unsigned WS_KV = WS_Q + (2u << 20);
constexpr unsigned WS_H = WS_KV + (4u << 20);
constexpr unsigned WS_W2 = WS_H + (7u << 20);
constexpr unsigned WS_WO = WS_W2 + 1792u * 1024u;
constexpr unsigned WS_O = WS_WO + 512u * 1024u;
constexpr unsigned WS_END = WS_O + (2u << 20);

constexpr unsigned U = 16384;
constexpr unsigned L_MISC = 9 * U, L_AUG = L_MISC, L_STAT = L_MISC + 4096, L_CNT = L_MISC + 6144, L_CONST = L_CNT + 128,
                   L_QF = L_MISC + 7168, L_KM = L_QF + 8192, L_TOTAL = 10 * U;
enum { C_DRAIN = 0, C_CS, C_KV1, C_KV2, C_HDRAIN, C_PROG  , C_WO, C_FX, C_G, C_AX, C_FB };
constexpr float BOUND_LIMIT = 40.f;
#define NBG 8

#define TS(i)
#define TSFLUSH()

#define f2u(x) __builtin_bit_cast(unsigned, (float)(x))
#define u2f(x) __builtin_bit_cast(float, (unsigned)(x))
#define fmaxf __builtin_fmaxf
DI f32x16 mfma32(u32x4 a, u32x4 b, f32x16 c) {
  return __builtin_amdgcn_mfma_f32_32x32x16_bf16(__builtin_bit_cast(bf16x8, a), __builtin_bit_cast(bf16x8, b), c, 0, 0, 0);
}
DI f32x4 fragsum(u32x4 sel, int selidx, u32x4 f0, u32x4 f1, f32x4 c) {
  const u32x8 b = __builtin_shufflevector(f0, f1, 0, 1, 2, 3, 4, 5, 6, 7);
  return __builtin_amdgcn_smfmac_f32_16x16x64_bf16(__builtin_bit_cast(bf16x8, sel), __builtin_bit_cast(bf16x16, b), c, selidx, 0, 0);
}
DI unsigned pk_bf16(float lo, float hi) {
  f32x2 f = {lo, hi};
  return __builtin_bit_cast(unsigned, __builtin_convertvector(f, bf16x2));
}
DI float rbf(float x) { return (float)(__bf16)x; }
DI float dot2(unsigned a, unsigned b, float c) {
  return __builtin_amdgcn_fdot2_f32_bf16(__builtin_bit_cast(bf16x2, a), __builtin_bit_cast(bf16x2, b), c, false);
}
DI float sq8(u32x4 v) { return dot2(v[0], v[0], dot2(v[1], v[1], dot2(v[2], v[2], dot2(v[3], v[3], 0.f)))); }
DI u32x4 lds_r128(unsigned addr) { return *(const LDS_AS u32x4*)(uintptr_t)addr; }
DI f32x4 lds_rf4(unsigned addr) { return *(const LDS_AS f32x4*)(uintptr_t)addr; }
DI float lds_rf(unsigned addr) { return *(const LDS_AS float*)(uintptr_t)addr; }
DI void lds_wu(unsigned addr, unsigned v) { *(LDS_AS unsigned*)(uintptr_t)addr = v; }
DI void lds_wf(unsigned addr, float v) { *(LDS_AS float*)(uintptr_t)addr = v; }
DI void lds_wf2(unsigned addr, f32x2 v) { *(LDS_AS f32x2*)(uintptr_t)addr = v; }
DI void lds_wf4(unsigned addr, f32x4 v) { *(LDS_AS f32x4*)(uintptr_t)addr = v; }
DI void lds_w128(unsigned addr, u32x4 v) { *(LDS_AS u32x4*)(uintptr_t)addr = v; }
DI u32x2 lds_tr(unsigned addr) {
  return __builtin_bit_cast(u32x2, __builtin_amdgcn_ds_read_tr16_b64_v4i16((LDS_AS s16x4*)(uintptr_t)addr));
}
DI u32x4 lds_trf(unsigned addr, unsigned step) {
  const u32x2 lo = lds_tr(addr), hi = lds_tr(addr + step);
  return (u32x4){lo[0], lo[1], hi[0], hi[1]};
}
DI float swap_add(float v) {
  auto r = __builtin_amdgcn_permlane32_swap(f2u(v), f2u(v), false, false);
  return u2f(r[0]) + u2f(r[1]);
}
DI float swap_max(float v) {
  auto r = __builtin_amdgcn_permlane32_swap(f2u(v), f2u(v), false, false);
  return fmaxf(u2f(r[0]), u2f(r[1]));
}
DI unsigned umax(unsigned a, unsigned b) { return a > b ? a : b; }
DI unsigned umax32(unsigned m) {
  m = umax(m, (unsigned)__builtin_amdgcn_update_dpp(0, (int)m, 0xB1, 0xf, 0xf, true));
  m = umax(m, (unsigned)__builtin_amdgcn_update_dpp(0, (int)m, 0x4E, 0xf, 0xf, true));
  m = umax(m, (unsigned)__builtin_amdgcn_update_dpp(0, (int)m, 0x141, 0xf, 0xf, true));
  m = umax(m, (unsigned)__builtin_amdgcn_update_dpp(0, (int)m, 0x140, 0xf, 0xf, true));
  auto r = __builtin_amdgcn_permlane16_swap(m, m, false, false);
  return umax(r[0], r[1]);
}
DI void glds(const void* base, unsigned voff, unsigned lds) {
  asm volatile("s_mov_b32 m0, %2\n\ts_nop 0\n\tglobal_load_lds_dwordx4 %0, %1" ::"v"(voff), "s"(base), "s"(lds) : "memory");
}
DI void glds4_dev(const void* base, unsigned voff, unsigned lds) {
  asm volatile("s_mov_b32 m0, %2\n\ts_nop 0\n\tglobal_load_lds_dwordx4 %0, %1 sc1\n\tglobal_load_lds_dwordx4 %0, %1 offset:1024 sc1\n\t"
               "global_load_lds_dwordx4 %0, %1 offset:2048 sc1\n\tglobal_load_lds_dwordx4 %0, %1 offset:3072 sc1" ::"v"(voff), "s"(base), "s"(lds) : "memory");
}
DI void glds2_dev(const void* base, unsigned voff, unsigned lds) {
  asm volatile("s_mov_b32 m0, %2\n\ts_nop 0\n\tglobal_load_lds_dwordx4 %0, %1 sc1\n\tglobal_load_lds_dwordx4 %0, %1 offset:1024 sc1" ::"v"(voff), "s"(base), "s"(lds) : "memory");
}
DI void st16_dev(void* base, unsigned voff, u32x4 v) {
  asm volatile("global_store_dwordx4 %0, %1, %2 sc1\n\ts_nop 1" ::"v"(voff), "v"(v), "s"(base) : "memory");
}
DI void st4_dev2(void* base, unsigned voff, unsigned v) {
  asm volatile("s_mov_b64 exec, %3\n\tglobal_store_dword %0, %1, %2 sc1\n\ts_mov_b64 exec, -1" ::"v"(voff), "v"(v), "s"(base), "s"(0x100000001ull) : "memory");
}
DI void st8_dev1(void* base, unsigned voff, u64 v) {
  asm volatile("s_mov_b64 exec, 1\n\tglobal_store_dwordx2 %0, %1, %2 sc1\n\ts_mov_b64 exec, -1" ::"v"(voff), "v"(v), "s"(base) : "memory");
}
DI unsigned pin(unsigned v) { asm volatile("" : "+v"(v)); return v; }
#define BARRIER() asm volatile("s_waitcnt lgkmcnt(0)\n\ts_barrier" ::: "memory")
#define WAITV(n) asm volatile("s_waitcnt vmcnt(" #n ")" ::: "memory")
#define WAITL() asm volatile("s_waitcnt lgkmcnt(0)" ::: "memory")
#define SB() __builtin_amdgcn_sched_barrier(0)
template <int C> DI void cnt_add() {
  asm volatile("s_waitcnt lgkmcnt(0)\n\ts_mov_b64 exec, 1\n\tds_add_u32 %0, %1 offset:%c2\n\ts_mov_b64 exec, -1" ::"v"(L_CNT), "v"(1u), "i"(4 * C) : "memory");
}
template <int C, int N, int SLEEP = 1> DI void cnt_wait() {
  unsigned t, i, v;
  asm volatile("s_mov_b32 %1, 0\n\ts_branch 2f\n\t1:\n\ts_sleep %c6\n\t2:\n\tds_read_b32 %0, %3 offset:%c4\n\ts_add_u32 %1, %1, 1\n\ts_waitcnt lgkmcnt(0)\n\t"
               "v_readfirstlane_b32 %2, %0\n\ts_bitcmp1_b32 %1, 20\n\ts_cselect_b32 %2, %c5, %2\n\ts_cmp_lt_u32 %2, %c5\n\ts_cbranch_scc1 1b"
               : "=&v"(t), "=&s"(i), "=&s"(v) : "v"(L_CNT), "i"(4 * C), "i"(N), "i"(SLEEP) : "memory", "scc");
}
template <int SLEEP = 2> DI void poll_flags(const char* ws, unsigned voff, u64 tag) {
  u64 v; unsigned i;
  asm volatile("s_mov_b32 %1, 0\n\ts_branch 2f\n\t1:\n\ts_sleep %c5\n\t2:\n\tglobal_load_dwordx2 %0, %2, %3 sc1\n\ts_add_u32 %1, %1, 1\n\ts_waitcnt vmcnt(0)\n\t"
               "v_cmp_ne_u64 vcc, %4, %0\n\ts_bitcmp1_b32 %1, 17\n\ts_cbranch_scc1 3f\n\ts_cbranch_vccnz 1b\n\t3:"
               : "=&v"(v), "=&s"(i) : "v"(voff), "s"(ws), "s"(tag), "i"(SLEEP) : "memory", "scc", "vcc");
}

#define CLOB_W "v56", "v57", "v58", "v59", "v60", "v61", "v62", "v63", "v64", "v65", "v66", "v67", "v68", "v69", "v70", "v71", "v72", "v73", "v74", "v75", \
               "v76", "v77", "v78", "v79", "v80", "v81", "v82", "v83", "v84", "v85", "v86", "v87", "v88", "v89", "v90", "v91", "v92", "v93", "v94", "v95"
#define P1_DX(J) "s_add_u32 m0, s57, (t&3)*16384+1024*" #J "\n\ts_nop 0\n\tglobal_load_lds_dwordx4 v[34+" #J "], s[48:49]\n\t"
#define P1_DX1 P1_DX(1) "s_add_u32 s48, s48, 128\n\ts_addc_u32 s49, s49, 0\n\t"
#define P1_DW "s_add_u32 m0, s56, 65536+(t&3)*8192\n\ts_nop 0\n\tglobal_load_lds_dwordx4 v36, s[50:51]\n\ts_add_u32 s50, s50, 64*3072\n\ts_addc_u32 s51, s51, 0\n\t"
#define P1_DQ ".if (t&1)==0\n\ts_add_u32 m0, s56, 98304+(t/2)*8192\n\ts_nop 0\n\tglobal_load_lds_dwordx4 v37, s[52:53]\n\ts_add_u32 s52, s52, 128*3072\n\ts_addc_u32 s53, s53, 0\n\t.endif\n\t"
#define P1_DF(J) "s_add_u32 m0, s57, (t&3)*16384+1024*" #J "\n\ts_nop 0\n\tglobal_load_lds_dwordx4 v[38+" #J "], s[54:55]\n\t"
#define P1_DF1 P1_DF(1) "s_add_u32 s54, s54, 128*3584\n\ts_addc_u32 s55, s55, 0\n\t"
#define P1_D0 ".if t<8\n\t" P1_DX(0) ".elseif t<11\n\t" P1_DF(0) ".endif\n\t"
#define P1_D1 ".if t<8\n\t" P1_DX1 ".elseif t<11\n\t" P1_DF1 ".endif\n\t"
#define P1_D2 ".if t<8\n\t" P1_DW ".endif\n\t"
#define P1_D3 ".if t<8\n\t" P1_DQ ".endif\n\t"
#define P1_WR(I, Z) ".if " #I "==0\n\t.set w, 64+4*" Z "\n\t.else\n\t.set w, 68+12*" Z "+4*" #I "\n\t.endif\n\t"
#define P1_RD(I) ".if s<8\n\t" P1_WR(I, "(s&1)") "ds_read_b128 v[128+16*s+4*" #I ":131+16*s+4*" #I "], v[24+" #I "] offset:(s&3)*16384\n\t" \
                 "ds_read_b64_tr_b16 v[w:w+1], v[28+" #I "] offset:(s&3)*8192\n\tds_read_b64_tr_b16 v[w+2:w+3], v[28+" #I "] offset:(s&3)*8192+512\n\t.endif\n\t"
#define P1_RQ(I) ".if s<8\n\t.set w, 48+8*" #I "+4*(s&1)\n\tds_read_b64_tr_b16 v[w:w+1], v[32+" #I "] offset:s*4096\n\tds_read_b64_tr_b16 v[w+2:w+3], v[32+" #I "] offset:s*4096+256\n\t.endif\n\t"
#define P1_MM(I) ".if s>0\n\t" P1_WR(I, "(p&1)") ".set xr, 128+16*p+4*" #I "\n\t.if (p==0)&&(" #I "==1)\n\tv_mfma_f32_32x32x16_bf16 v[96:111], v[w:w+3], v[xr:xr+3], 0\n\t.else\n\t" \
                 "v_mfma_f32_32x32x16_bf16 v[96:111], v[w:w+3], v[xr:xr+3], v[96:111]\n\t.endif\n\t.endif\n\t"
#define P1_MQ(I) ".if s>0\n\t.set w, 48+8*" #I "+4*(p&1)\n\t.set xr, 128+16*p+4*" #I "\n\t.if (p+" #I ")==0\n\tv_mfma_f32_32x32x16_bf16 v[112:127], v[w:w+3], v[xr:xr+3], 0\n\t.else\n\t" \
                 "v_mfma_f32_32x32x16_bf16 v[112:127], v[w:w+3], v[xr:xr+3], v[112:127]\n\t.endif\n\t.endif\n\t"
#define P1_TS(B)
#define P1_TSOP
#define P1_TSCL
#define P1_SQ(J) ".if s>0\n\t.set xr, 128+16*p+" #J "\n\tv_and_b32 v4, v8, v[xr]\n\tv_lshlrev_b32 v5, 16, v[xr]\n\tv_fmac_f32 v45, v4, v4\n\tv_fmac_f32 v46, v5, v5\n\t.endif\n\t"
#define P1_HEAD_TEXT \
  "s_setprio 3\n\t.set t, 0\n\t" P1_D0 P1_D1 P1_D2 P1_D3 "s_setprio 2\n\t.set t, 1\n\t" P1_D0 P1_D1 P1_D2 P1_D3 "s_setprio 0"
#define P1_TEXT \
  "v_mov_b32 v8, 0xffff0000\n\t.set s, 0\n\t.rept 9\n\t.set p, s-1\n\t.set t, s+3\n\t" P1_TS("44") \
  ".if s==0\n\ts_waitcnt vmcnt(6) lgkmcnt(0)\n\t.elseif s==1\n\ts_waitcnt vmcnt(10) lgkmcnt(0)\n\t.elseif s<6\n\ts_waitcnt vmcnt(7) lgkmcnt(0)\n\t.elseif s==6\n\ts_waitcnt vmcnt(5) lgkmcnt(0)\n\t" \
  ".elseif s==7\n\ts_waitcnt vmcnt(4) lgkmcnt(0)\n\t.else\n\ts_waitcnt lgkmcnt(0)\n\t.endif\n\t.if s<8\n\ts_barrier\n\t.endif\n\t" P1_TS("54") \
  ".if s==0\n\t" P1_RD(0) P1_RD(1) P1_RD(2) P1_RD(3) P1_RQ(0) P1_RQ(1) ".set t, 2\n\t" P1_D0 P1_D1 P1_D2 P1_D3 ".set t, 3\n\t" P1_D0 P1_D1 P1_D2 P1_D3 ".else\n\t" \
  P1_MM(1) P1_D0 P1_RD(1) P1_SQ(0) P1_MM(2) P1_D1 P1_RD(2) P1_SQ(1) \
  ".if (s&1)==0\n\tv_smfmac_f32_16x16x64_bf16 v[12:15], v[40:43], v[64:71], v44\n\tv_smfmac_f32_16x16x64_bf16 v[16:19], v[40:43], v[48:55], v44\n\t.endif\n\t" \
  P1_MM(3) P1_D2 P1_RD(3) P1_SQ(2) P1_MM(0) P1_D3 P1_RD(0) P1_SQ(3) P1_MQ(0) P1_RQ(0) P1_RQ(1) P1_SQ(4) P1_SQ(5) P1_MQ(1) P1_SQ(6) \
  "v_smfmac_f32_16x16x64_bf16 v[20:23], v[40:43], v[128+16*p:135+16*p], v44\n\t" P1_SQ(7) \
  ".endif\n\t.set s, s+1\n\t.endr\n\ts_nop 7"
#define TL_W(N) ".if (" N "&3)==0\n\t.set w, 56+((" N "/4)&3)*4\n\t.else\n\t.set w, 72+(((" N "/4)*3+(" N "&3)-1)%%6)*4\n\t.endif\n\t"
#define TL_RD ".set n, f+5\n\t.if n<64\n\t.set o, ((n/4)&7)*8192\n\t.set b, 24+(n/32)*4+(n&3)\n\t" TL_W("n") \
              "ds_read_b64_tr_b16 v[w:w+1], v[b] offset:o\n\tds_read_b64_tr_b16 v[w+2:w+3], v[b] offset:o+512\n\t.endif\n\t"
#define TL_MM(A, C) ".if f<59\n\ts_waitcnt lgkmcnt(10)\n\t.else\n\ts_waitcnt lgkmcnt(2*(63-f))\n\t.endif\n\t" TL_W("f") ".set x, 128+(f&31)*4\n\t" \
              ".if (f&31)==0\n\tv_mfma_f32_32x32x16_bf16 v[" A "], v[w:w+3], v[x:x+3], 0\n\t.else\n\tv_mfma_f32_32x32x16_bf16 v[" A "], v[w:w+3], v[x:x+3], v[" A "]\n\t.endif\n\t" \
              ".if (f&7)==4\n\tv_smfmac_f32_16x16x64_bf16 v[" C "], v[40:43], v[w-4:w+3], v44\n\t.endif\n\t"
#define TILE_SUB_TEXT \
  ".Ltile_sub:\n\t.set f, -5\n\t.rept 5\n\t" TL_RD ".set f, f+1\n\t.endr\n\t" \
  ".set k, 16\n\t.rept 8\n\tv_mov_b32 v[k], 0\n\t.set k, k+1\n\t.endr\n\t" \
  ".rept 32\n\t" TL_RD TL_MM("96:111", "16:19") ".set f, f+1\n\t.endr\n\t" \
  "s_cmp_eq_u32 s46, 0\n\ts_cbranch_scc1 .Ltile_ret\n\ts_cmp_eq_u32 s46, 1\n\ts_cbranch_scc1 .Ltile_two\n\t" \
  "v_mov_b32 v45, %c0\n\tv_mov_b32 v46, 1\n\ts_waitcnt vmcnt(0)\n\ts_mov_b64 exec, 1\n\tds_add_u32 v45, v46\n\ts_mov_b64 exec, -1\n\t" \
  ".Ltile_two:\n\t.rept 32\n\t" TL_RD TL_MM("112:127", "20:23") ".set f, f+1\n\t.endr\n\t" \
  ".Ltile_ret:\n\ts_waitcnt lgkmcnt(0)\n\ts_nop 9\n\ts_setpc_b64 s[44:45]\n\t"
#define TILES(BASES, MODE)   \
  asm volatile("s_cmp_eq_u32 s46, 3\n\ts_cbranch_scc1 1f\n\ts_call_b64 s[44:45], .Ltile_sub\n\t1:" : "+{v[96:111]}"(accA), "+{v[112:127]}"(accB), "={v[16:23]}"(cst) \
               : "{v[128:159]}"(X0), "{v[160:191]}"(X1), "{v[192:223]}"(X2), "{v[224:255]}"(X3), "{v[24:31]}"(BASES), "{v[40:43]}"(sel), "{v44}"(selidx), \
                 "{s46}"(MODE) : "memory", "scc", "s44", "s45", "v45", "v46", CLOB_W)

#define E(r) "v_exp_f32 v" #r ", v" #r "\n\t"
#define QK(Dst, Q) "v_mfma_f32_32x32x16_bf16 v[" Dst "], v[144:147], v[" Q "], v[112:127]\n\t"
#define PV(O, V, P) "v_smfmac_f32_32x32x32_bf16 v[" O "], v[" V "], v[" P "], v164\n\t"
#define UA(OP, VP1, QN, O, V0, X0, X1) E(16) E(17) PV(OP, VP1, "40:47") E(18) E(19) E(20) X0 QK("32:47", QN) X1 E(21) E(22) E(23) E(24) E(25) E(26) PV(O, V0, "16:23") E(27) E(28) E(29) E(30) E(31)
#define UB(OP, VP1, QN, O, V0, X0, X1) E(32) E(33) PV(OP, VP1, "24:31") E(34) E(35) E(36) X0 QK("16:31", QN) X1 E(37) E(38) E(39) E(40) E(41) E(42) PV(O, V0, "32:39") E(43) E(44) E(45) E(46) E(47)
#define TR(D, OFF) "ds_read_b64_tr_b16 v[" D "], v166 offset:" OFF "\n\t"
#define KRD(OFF) "ds_read_b128 v[144:147], v165 offset:" OFF "\n\t"
#define WT "s_waitcnt lgkmcnt(0)\n\t"
#define VADV "v_add_u32 v166, v167, v166\n\t"
#define TILE4(VP1, V0, V1, N0, N1, N2, N3, OK)                                          \
  UA("96:111", VP1, "132:135", "48:63", V0, VADV, TR(N0, "0") TR(N1, "128"))            \
  UB("48:63", V1, "136:139", "64:79", V0, "", TR(N2, "512") TR(N3, "640"))              \
  UA("64:79", V1, "140:143", "80:95", V0, "", KRD(OK))                                  \
  UB("80:95", V1, "128:131", "96:111", V0, WT, "")

#if defined(__HIP_DEVICE_COMPILE__)
#define NOPK __attribute__((target("no-packed-fp32-ops")))
#else
#define NOPK
#endif
#define LAM NOPK __attribute__((always_inline))
__global__ NOPK __launch_bounds__(512, 2) void fused_kernel(const char* __restrict__ x, const char* __restrict__ wqkv, const char* __restrict__ wo,
                                                       const char* __restrict__ w1, const char* __restrict__ b1, const char* __restrict__ w2,
                                                       const char* __restrict__ b2, char* __restrict__ out, char* __restrict__ ws) {
  extern __shared__ __attribute__((aligned(16))) char smem[];
  const int wid = __builtin_amdgcn_readfirstlane(threadIdx.x >> 6);
  const int xcd = blockIdx.x & 7, idx = blockIdx.x >> 3;
  const int g = wid >> 2, tg = wid & 3;
  int lane, r, hh, ch, q4, p4, p4s;
  unsigned l16;
#define RELANE() do { asm volatile("v_mbcnt_lo_u32_b32 %0, -1, 0\n\tv_mbcnt_hi_u32_b32 %0, -1, %0" : "=v"(lane)); __builtin_assume(lane >= 0 && lane < 64); \
    r = lane & 31; hh = lane >> 5; ch = (lane >> 4) & 1; q4 = (lane & 15) >> 2; p4 = lane & 3; l16 = lane * 16; \
    p4s = ((p4 & 1) << 1) | (p4 >> 1);   } while (0)
  RELANE();
  const int rs = (r & 19) | ((r & 4) << 1) | ((r & 8) >> 1);
  TS(0);
  const int mt = (xcd >> 1) * 4 + (idx & 3), np = (xcd & 1) * 8 + (idx >> 2);
  const bool isk = np < 8, even = !(np & 1);
  const int f01 = even ? 2 * np : 32 + (np & ~1), f2 = even ? 2 * np + 2 : 48 + (np >> 1);
  const unsigned vp = ((lane & 7) ^ ((lane >> 2) & 4)) << 4, vf = (lane >> 3) * 3584 + vp;
  u64 sx = (u64)(x + (size_t)(mt * 128 + 16 * wid) * 1024), sw = (u64)(wqkv + 1024 + np * 128 + (size_t)(8 * wid) * 3072),
      sq = (u64)(wqkv + np * 64 + (size_t)(16 * wid) * 3072), sf = (u64)(w1 + f01 * 64 + (size_t)(16 * wid) * 3584);
  const unsigned vx0 = (lane >> 3) * 1024 + (((lane & 7) ^ (lane >> 4)) << 4);
  const u32x2 vx = {vx0, (vx0 ^ 64) + 8192}, vwq = {(lane >> 3) * 3072 + vp, (lane >> 2) * 3072u + (lane & 3) * 16}, vff = {vf, vf + 8 * 3584};
  asm volatile(P1_HEAD_TEXT : "+{s[48:49]}"(sx), "+{s[50:51]}"(sw), "+{s[52:53]}"(sq)
               : "{v[34:35]}"(vx), "{v[36:37]}"(vwq), "{s56}"(wid * 1024), "{s57}"(wid * 2048) : "memory", "scc");
  unsigned bias0, bias1, pf_junk;
  {
    const unsigned bo = ((g ? f2 : f01) * 32 + rs) * 2, bo1 = (g && !even) ? bo : bo + 64;
    u64 pc;
    unsigned len;
    asm volatile("s_getpc_b64 %0\n\t.Lpf_base:\n\ts_mov_b32 %1, .Ltile_ret-.Lpf_base" : "=s"(pc), "=s"(len));
    const u64 p01 = (tg & 1) ? (u64)wqkv : (u64)x, p23 = (tg & 1) ? pc : (u64)w1, pb = (tg & 2) ? p23 : p01;
    const unsigned n01 = (tg & 1) ? 6144u : 8192u, n23 = (tg & 1) ? 8192u : 7168u, nb = (tg & 2) ? n23 : n01;
    const unsigned o0 = (tg == 3 ? (idx & 3) : blockIdx.x) * nb, lim = tg == 3 ? len : o0 + nb - 128;
    unsigned co = o0 + (g ? lane * 128 : 0);
    co = co < lim ? co : lim;
    u64 pbb = pb;
    if (wid == 0) { pbb = (u64)ws; co = (lane < 36 ? lane : 0) * 524288 + 20480; }
    if (wid == 1) { pbb = (u64)out; co = (lane & 7) * 262144; }
    if (wid == 2) { pbb = (u64)w2; co = (lane & 7) * 229376; }
    if (wid == 3) { pbb = (u64)wo; co = (lane & 7) * 65536; }
    asm volatile("global_load_ubyte %2, %5, %6 sc1\n\tglobal_load_ushort %0, %3, %7\n\tglobal_load_ushort %1, %4, %7"
                 : "=&{v10}"(bias0), "=&{v11}"(bias1), "=&{v47}"(pf_junk) : "v"(bo), "v"(bo1), "v"(co), "s"(pbb), "s"(b1) : "memory");
  }
  u64 epoch;
  asm volatile("s_load_dwordx2 %0, %1, 0x0" : "=s"(epoch) : "s"(ws) : "memory");
  if (wid == 4) {
    lds_wu(L_CNT + r * 4, 0u);
    lds_w128(L_CONST + (lane < 48 ? lane : 47) * 16, (u32x4){0x3f80u, 0u, 0u, 0u});
  }
  const unsigned ones = 0x3f803f80u;
  const bool selon = (lane & 3) < 2, selhi = (lane >> 2) & 1;
  const u32x4 sel = {selon && !selhi ? ones : 0u, selon && !selhi ? ones : 0u, selon && selhi ? ones : 0u, selon && selhi ? ones : 0u};
  const int selidx = (lane & 3) == 0 ? 0x4444 : 0xEEEE;
  const unsigned wl = (8 * hh + q4) * 64 + ch * 32 + p4s * 8;
  const unsigned wp = (8 * hh + q4) * 128 + (q4 >> 1) * 64 + ch * 32 + p4s * 8;
  const int rot = tg ^ (2 * g);
  u32x4 wa;
  u32x32 X0, X1, X2, X3;
  f32x16 accA, accB;
  auto put_cs = [&](float c, int aug, unsigned bias) LAM {
    const float chi = rbf(c);
    const unsigned cw = pin(pk_bf16(chi, c - chi)), bw = pin(tg == 0 ? bias : 0u);
    lds_wu(L_AUG + aug * 1024 + l16 + tg * 4, hh ? bw : cw);
  };
  {
    f32x4 cs = {0.f, 0.f, 0.f, 0.f}, cq = cs, xs = cs;
    float xq0 = 0.f, xq1 = 0.f;
    const int row = 32 * tg + r;
    u32x4 xa;
#pragma unroll
    for (int i = 0; i < 4; ++i) {
      xa[i] = row * 128 + (((2 * (i ^ rot) + hh) ^ ((row >> 1) & 7)) << 4);
      wa[i] = pin(((wp + (i ^ rot) * 2048) ^ (g * 64)) + 4 * U);
    }
    const u32x2 qa = {(wl ^ (g * 32)) + rot * 1024 + 6 * U, (wl ^ (g * 32)) + (rot ^ 1) * 1024 + 6 * U};
    TS(1);
    asm volatile(P1_TEXT
                 : "={v[128:159]}"(X0), "={v[160:191]}"(X1), "={v[192:223]}"(X2), "={v[224:255]}"(X3), "={v[96:111]}"(accA), "={v[112:127]}"(accB),
                   "+{v[12:15]}"(cs), "+{v[16:19]}"(cq), "+{v[20:23]}"(xs), "+{v45}"(xq0), "+{v46}"(xq1), "+{s[48:49]}"(sx), "+{s[50:51]}"(sw), "+{s[52:53]}"(sq),
                   "+{s[54:55]}"(sf), "+s"(epoch), "+{v10}"(bias0), "+{v11}"(bias1), "+{v47}"(pf_junk) P1_TSOP
                 : "{v[24:27]}"(xa), "{v[28:31]}"(wa), "{v[32:33]}"(qa), "{v[34:35]}"(vx), "{v[36:37]}"(vwq), "{v[38:39]}"(vff), "{s56}"(wid * 1024),
                   "{s57}"(wid * 2048), "{v[40:43]}"(sel), "{v44}"(selidx)
                 : "memory", "scc", "v4", "v5", "v8", "v48", "v49", "v50", "v51", "v52", "v53", "v54", "v55", CLOB_W P1_TSCL);
    TS(2);
    RELANE();
    lds_wf4(8 * U + wid * 2048 + l16, (f32x4){accB[8], accB[9], accB[10], accB[11]});
    lds_wf4(8 * U + wid * 2048 + 1024 + l16, (f32x4){accB[12], accB[13], accB[14], accB[15]});
    lds_wf2(L_STAT + (32 * tg + r) * 16 + g * 8, (f32x2){xs[0] + xs[1], swap_add(xq0 + xq1)});
    put_cs(cs[0] + cs[1], g, 0u);
    put_cs(cq[0] + cq[1], 2 + g, 0u);
  }
  BARRIER();
  TS(3);
  float rstd;
  u32x4 xaug;
  {
    const f32x4 st = lds_rf4(L_STAT + (32 * tg + r) * 16);
    const float mean = (st[0] + st[2]) * (1.f / 512.f);
    const float var = fmaxf((st[1] + st[3]) * (1.f / 512.f) - mean * mean, 0.f);
    const float sd = rbf(__builtin_amdgcn_sqrtf(rbf(rbf(var) + 1e-5f)));
    rstd = __builtin_amdgcn_rcpf(sd);
    const unsigned nm = pin(pk_bf16(-mean, -mean)), sw_ = pin(pk_bf16(sd, 0.f)), nz = hh ? 0u : nm;
    xaug = (u32x4){hh ? sw_ : nm, nz, nz, nz};
  }
  glds((const void*)sf, vff[0], 3 * U + wid * 2048);
  glds((const void*)sf, vff[1], 3 * U + wid * 2048 + 1024);
  const int tt = mt * 4 + tg;
  auto finish = [&](f32x16 a, int aug, bool relu2, u32x4& o0, u32x4& o1) LAM {
    a = mfma32(lds_r128(L_AUG + aug * 1024 + l16), xaug, a);
#pragma unroll
    for (int i = 0; i < 8; ++i) {
      float u = a[2 * i] * rstd, v = a[2 * i + 1] * rstd;
      if (relu2) { u = fmaxf(u, 0.f); v = fmaxf(v, 0.f); u *= u; v *= v; }
      if (i < 4) o0[i] = pk_bf16(u, v); else o1[i - 4] = pk_bf16(u, v);
    }
  };
  {
    u32x4 o0, o1, oq;
    const f32x16 a = mfma32(lds_r128(L_AUG + (2 + g) * 1024 + l16), xaug, accB);
    const f32x4 r0 = lds_rf4(8 * U + (wid ^ 4) * 2048 + l16), r1 = lds_rf4(8 * U + (wid ^ 4) * 2048 + 1024 + l16);
    finish(accA, g, false, o0, o1);
    const int head = (np & 7) * 4 + g * 2;
    char* kv = ws + WS_KV + ((size_t)head * 64 + tt) * 2048;
    const unsigned vo = isk ? l16 : 1024 + r * 32 + hh * 16;
    st16_dev(kv, vo, o0);
    st16_dev(kv + 131072, vo, o1);
    const float fq = rstd * (0.25f * 1.4426950408889634f);
    oq[0] = pk_bf16((a[0] + r0[0]) * fq, (a[1] + r0[1]) * fq);
    oq[1] = pk_bf16((a[2] + r0[2]) * fq, (a[3] + r0[3]) * fq);
    oq[2] = pk_bf16((a[4] + r1[0]) * fq, (a[5] + r1[1]) * fq);
    oq[3] = pk_bf16((a[6] + r1[2]) * fq, (a[7] + r1[3]) * fq);
    st16_dev(ws + WS_Q + ((size_t)(2 * np + g) * 64 + tt) * 1024, l16, oq);
    if (isk) {
      const float n0 = swap_add(sq8(o0)), n1 = swap_add(sq8(o1));
      const unsigned m = umax32(f2u(hh ? n1 : n0));
      st4_dev2(ws + WS_KMAX + (head * 64 + tt) * 4, hh * 256, m);
    }
  }
  const u64 tag = [&]() LAM { u64 z = epoch + 0x9E3779B97F4A7C15ull; z = (z ^ (z >> 30)) * 0xBF58476D1CE4E5B9ull; z = (z ^ (z >> 27)) * 0x94D049BB133111EBull; return z ^ (z >> 31); }();
  TS(4);
  if (isk) WAITV(4); else WAITV(3);
  BARRIER();
  TS(5);
  u32x4 hp[4];
  const int head = xcd * 4 + (idx >> 3), qb = idx & 7;
  const int m2 = xcd * 4 + (idx & 3), n2 = idx >> 2;
  char* hb = ws + WS_H + (size_t)tt * 112 * 1024;
  const int fr0 = (blockIdx.x & 31) * 9 + tg * 3;
  const char* kv = ws + WS_KV + (size_t)head * 131072;
  f32x8 cst;
  {
    const unsigned x0 = g ? (even ? 64 : ((f2 & 1) ^ 1) * 64) : 0, x1 = g ? 0 : 64, d = g ? U : 0u - 4 * U;
    const u32x8 bases = {(wa[0] ^ x0) + d, (wa[1] ^ x0) + d, (wa[2] ^ x0) + d, (wa[3] ^ x0) + d, (wa[0] ^ x1) + d, (wa[1] ^ x1) + d, (wa[2] ^ x1) + d, (wa[3] ^ x1) + d};
    TILES(bases, g ? 3 : 2);
    if (!g) {
      put_cs(cst[0] + cst[1], 0, bias0);
      put_cs(cst[4] + cst[5], 1, bias1);
    } else {
      const char* wq = w1 + (f2 & ~1) * 64 + (size_t)(16 * tg) * 3584;
#pragma unroll 1
      for (int i = 0; i < 8; ++i) {
        glds(wq + (size_t)i * 64 * 3584, vff[0], 5 * U + i * 8192 + tg * 2048);
        glds(wq + (size_t)i * 64 * 3584, vff[1], 5 * U + i * 8192 + tg * 2048 + 1024);
      }
      TS(6);
      WAITV(16);
      cnt_add<C_DRAIN>();
      TS(7);
      if (tg == 0) {
        cnt_wait<C_DRAIN, 8>();
        st8_dev1(ws + WS_FA, (np * 16 + mt) * 8, tag);
        WAITV(1);
      } else {
        WAITV(0);
      }
    }
    TS(8);
    BARRIER();
    TS(9);
    if (!g) __builtin_amdgcn_s_setprio(2);
    TILES(bases, g ? (even ? 1 : 0) : 3);
  }
  RELANE();
  const int qg = tg & 1, kh = tg >> 1;
  u32x4 qf[4];
  unsigned kmu;
  if (g) {
    TS(10);
    __builtin_amdgcn_s_setprio(3);
    put_cs(cst[0] + cst[1], 2, bias0);
    put_cs(cst[4] + cst[5], 3, bias1);
    cnt_add<C_CS>();
    cnt_wait<C_CS, 4>();
    asm volatile("" : "={v[128:131]}"(qf[0]), "={v[132:135]}"(qf[1]), "={v[136:139]}"(qf[2]), "={v[140:143]}"(qf[3]), "={v144}"(kmu));
  } else {
  {
    const unsigned fkv = pin((head >> 2) * 16 + ch * 128 + (lane & 15)), fq = pin((head >> 1) * 16 + 2 * qb + (lane & 1));
    poll_flags<1>(ws, WS_FA + (hh ? fq : fkv) * 8, tag);
  }
  TS(10);
  {
    const char* qp = ws + WS_Q + ((size_t)head * 64 + qb * 8 + 4 * qg) * 1024;
    asm volatile("global_load_dwordx4 %0, %5, %7 sc1\n\tglobal_load_dwordx4 %1, %5, %7 offset:1024 sc1\n\tglobal_load_dwordx4 %2, %6, %7 sc1\n\t"
                 "global_load_dwordx4 %3, %6, %7 offset:1024 sc1\n\tglobal_load_dword %4, %8, %9 sc1"
                 : "=&{v[128:131]}"(qf[0]), "=&{v[132:135]}"(qf[1]), "=&{v[136:139]}"(qf[2]), "=&{v[140:143]}"(qf[3]), "=&{v144}"(kmu)
                 : "v"(l16 + kh * 2048), "v"(l16 + (1 - kh) * 2048), "s"(qp), "v"(lane * 4), "s"(ws + WS_KMAX + head * 256) : "memory");
    glds4_dev(kv + kh * 4 * U + qg * 4096, l16, kh * 4 * U + qg * 4096);
    glds4_dev(kv + kh * 4 * U + 8192 + qg * 4096, l16, kh * 4 * U + 8192 + qg * 4096);
  }
  }
  finish(accA, 2 * g, true, hp[0], hp[1]);
  finish(accB, 2 * g + 1, true, hp[2], hp[3]);
  asm volatile("" : "+v"(hp[0]), "+v"(hp[1]), "+v"(hp[2]), "+v"(hp[3]));
  TS(11);
  RELANE();
  if (g) {
    cnt_wait<C_KV1, 4>();
    TS(12);
#pragma unroll
    for (int i = 1; i < 4; ++i) { glds4_dev(kv + i * U + tg * 4096, l16, i * U + tg * 4096); glds4_dev(kv + (4 + i) * U + tg * 4096, l16, (4 + i) * U + tg * 4096); }
    if (tg < 3) {
#pragma unroll
      for (int i = 0; i < 3; ++i) {
        const int band = (fr0 + i) >> 1, nt = (blockIdx.x >> 5) * 2 + ((fr0 + i) & 1);
        glds((band < 112 ? w2 + (size_t)band * 16384 : wo + (size_t)(band - 112) * 16384) + nt * 64, (lane >> 2) * 1024 + (lane & 3) * 16, 8 * U + tg * 4096 + i * 1024);
      }
      WAITV(3);
    } else {
      WAITV(0);
    }
    cnt_add<C_KV2>();
    TS(13);
#pragma unroll
    for (int i = 0; i < 4; ++i) st16_dev(hb + (size_t)(f2 * 2 + ((even || i < 2) ? i : i - 2)) * 1024, l16, (even || i < 2) ? hp[i] : hp[i - 2]);
    if (tg < 3) {
      WAITV(4);
#pragma unroll
      for (int i = 0; i < 3; ++i) {
        const int band = (fr0 + i) >> 1, nt = (blockIdx.x >> 5) * 2 + ((fr0 + i) & 1);
        const u32x4 v = lds_trf(8 * U + tg * 4096 + i * 1024 + (8 * hh + q4) * 64 + ch * 32 + p4s * 8, 256);
        st16_dev(ws + (band < 112 ? WS_W2 + (nt * 112 + band) * 1024 : WS_WO + (nt * 32 + band - 112) * 1024), l16, v);
      }
    }
    WAITV(0);
    cnt_add<C_HDRAIN>();
    TS(14);
    if (tg == 0) {
      cnt_wait<C_HDRAIN, 8, 8>();
      st8_dev1(ws + WS_FB, blockIdx.x * 8, tag);
      TS(15);
      const unsigned fh = pin(((m2 >> 3) * 2 + ((lane >> 3) & 1)) + 8 * (((m2 >> 1) & 3) + 4 * (lane & 7))), fw = pin(32 * n2 + r);
      poll_flags<8>(ws, WS_FB + (hh ? fw : fh) * 8, tag);
      cnt_add<C_FB>();
    } else {
      cnt_wait<C_FB, 1, 8>();
    }
    TS(16);
    const auto rsrc = __builtin_amdgcn_make_buffer_rsrc(ws, 0, WS_END, 0x00020000);
    const unsigned ho[2] = {WS_H + ((m2 * 2 + (tg >> 1)) * 112 + tg * 28) * 1024, WS_H + ((m2 * 2 + 1 - (tg >> 1)) * 112 + tg * 28) * 1024};
    const unsigned wo2[2] = {WS_W2 + ((n2 * 2 + (tg & 1)) * 112 + tg * 28) * 1024, WS_W2 + ((n2 * 2 + 1 - (tg & 1)) * 112 + tg * 28) * 1024};
    constexpr int DEP = 9;
    u32x4 hf[DEP][2], wf[DEP][2];
    auto ld = [&](int s) LAM {
#pragma unroll
      for (int u = 0; u < 2; ++u) {
        hf[s % DEP][u] = __builtin_amdgcn_raw_buffer_load_b128(rsrc, l16 + (s & 3) * 1024, ho[u] + (s & ~3) * 1024, 16);
        wf[s % DEP][u] = __builtin_amdgcn_raw_buffer_load_b128(rsrc, l16 + (s & 3) * 1024, wo2[u] + (s & ~3) * 1024, 16);
      }
    };
    {
      constexpr int S0 = NBG + DEP, NL = (28 - S0) * 8;
      unsigned junk[8];
#pragma unroll
      for (int i = 0; i < 8; ++i) {
        const unsigned li = (i & 1) * 64 + lane;
        junk[i] = __builtin_amdgcn_raw_buffer_load_b32(rsrc, (li < NL ? li : 0) * 128, ((i >> 1) < 2 ? ho[(i >> 1) & 1] : wo2[(i >> 1) & 1]) + S0 * 1024, 16);
      }
#pragma unroll
      for (int i = 0; i < 8; ++i) asm volatile("" ::"v"(junk[i]));
    }
    cnt_wait<C_PROG, 4, 16>();
#pragma unroll
    for (int i = 0; i < 4; ++i)
      glds4_dev(ws + WS_WO + ((n2 * 2 + (i >> 1)) * 32 + tg * 8 + (i & 1) * 4) * 1024, l16, (i >> 1) * 4 * U + (tg * 8 + (i & 1) * 4) * 1024);
    WAITV(0);
    cnt_add<C_WO>();
    TS(17);
    f32x16 fa[2][2];
#pragma unroll
    for (int a = 0; a < 2; ++a)
#pragma unroll
      for (int b = 0; b < 2; ++b)
#pragma unroll
        for (int i = 0; i < 16; ++i) fa[a][b][i] = 0.f;
#pragma unroll
    for (int s = 0; s < DEP; ++s) ld(s);
    SB();
    __builtin_amdgcn_s_setprio(0);
#pragma unroll
    for (int s = 0; s < 28; ++s) {
      if (s == NBG) { TS(30); asm volatile("s_barrier" ::: "memory"); TS(31); }
#pragma unroll
      for (int a = 0; a < 2; ++a)
#pragma unroll
        for (int b = 0; b < 2; ++b) fa[a][b] = mfma32(wf[s % DEP][b], hf[s % DEP][a], fa[a][b]);
      SB();
      if (s + DEP < 28) ld(s + DEP);
      SB();
    }
    TS(32);
    RELANE();
    const int n0 = n2 * 64 + (tg & 1) * 32 + 8 * hh;
    const u32x4 bb0 = *(const u32x4*)(b2 + n0 * 2), bb1 = *(const u32x4*)(b2 + n0 * 2 + 32);
    auto fslot = [&](int dest, int d) LAM { const int k = dest * 3 + d - 1; return (k < 8 ? 2 * U + k * 4096 : 6 * U + (k - 8) * 4096) + l16; };
#pragma unroll
    for (int d = 1; d < 4; ++d) {
      const f32x16 v = fa[d >> 1][d & 1];
      const unsigned a = fslot(tg ^ d, d);
#pragma unroll
      for (int j = 0; j < 4; ++j) lds_wf4(a + j * 1024, (f32x4){v[4 * j], v[4 * j + 1], v[4 * j + 2], v[4 * j + 3]});
    }
    cnt_add<C_FX>();
    TS(33);
    cnt_wait<C_FX, 4>();
    TS(34);
    f32x16 z = fa[0][0];
#pragma unroll
    for (int d = 1; d < 4; ++d)
#pragma unroll
      for (int j = 0; j < 4; ++j) {
        const f32x4 v = lds_rf4(fslot(tg, d) + j * 1024);
#pragma unroll
        for (int i = 0; i < 4; ++i) z[4 * j + i] += v[i];
      }
#pragma unroll
    for (int i = 0; i < 16; ++i) {
      const unsigned bw = i < 8 ? bb0[i >> 1] : bb1[(i - 8) >> 1];
      const float y = z[i] + u2f((i & 1) ? (bw & 0xffff0000u) : (bw << 16));
      z[i] = y * __builtin_amdgcn_rcpf(1.f + __builtin_amdgcn_exp2f(y * -1.4426950408889634f));
    }
#pragma unroll
    for (int j = 0; j < 4; ++j) lds_wf4(7 * U + tg * 4096 + j * 1024 + l16, (f32x4){z[4 * j], z[4 * j + 1], z[4 * j + 2], z[4 * j + 3]});
    cnt_add<C_G>();
    TS(35);
    TSFLUSH();
    return;
  }
  RELANE();
  {
    asm volatile("s_waitcnt vmcnt(8)" : "+{v[128:131]}"(qf[0]), "+{v[132:135]}"(qf[1]), "+{v[136:139]}"(qf[2]), "+{v[140:143]}"(qf[3]), "+{v144}"(kmu)::"memory");
  }
  TS(13);
  kmu = umax32(kmu);
  { auto t2 = __builtin_amdgcn_permlane32_swap(kmu, kmu, false, false); kmu = umax(t2[0], t2[1]); }
  const unsigned qn = umax(umax(f2u(swap_add(sq8(qf[0]))), f2u(swap_add(sq8(qf[1])))),
                           umax(f2u(swap_add(sq8(qf[2]))), f2u(swap_add(sq8(qf[3])))));
  const float bound = __builtin_amdgcn_sqrtf(u2f(qn) * u2f(kmu)) * 1.001f;
  f32x16 o[4];
  float mx[4];
  unsigned ka = kh * 4 * U + lane * 16;
  const unsigned vac = pin(L_CONST + (p4 >> 1) * 8), vad = pin(kh * 4 * U + 1024 + (8 * hh + q4) * 32 + p4s * 8);
  unsigned va = ch ? vac : vad;
  const unsigned vstep = ch ? 0u : 2048u;
#pragma unroll
  for (int u = 0; u < 4; ++u)
#pragma unroll
    for (int i = 0; i < 16; ++i) o[u][i] = 0.f;
  asm volatile("" : "+v"(o[0]), "+v"(o[1]), "+v"(o[2]), "+v"(o[3]), "+v"(va));
  WAITV(0);
  cnt_add<C_KV1>();
  cnt_wait<C_KV1, 4>();
  TS(14);
  char* hst = hb + (size_t)f01 * 2048;
  if (__builtin_amdgcn_ballot_w64(bound <= BOUND_LIMIT) == ~0ull) {
    f32x16 cneg;
#pragma unroll
    for (int i = 0; i < 16; ++i) cneg[i] = -bound;
    const u32x8 q01 = __builtin_shufflevector(qf[0], qf[1], 0, 1, 2, 3, 4, 5, 6, 7), q23 = __builtin_shufflevector(qf[2], qf[3], 0, 1, 2, 3, 4, 5, 6, 7);
    asm volatile(
        "v_mov_b32 v164, 0xdddd\n\t"
        KRD("0") TR("148:149", "0") TR("150:151", "128") TR("152:153", "512") TR("154:155", "640")
        "v_mov_b32 v40, 0\n\tv_mov_b32 v41, 0\n\tv_mov_b32 v42, 0\n\tv_mov_b32 v43, 0\n\t"
        "v_mov_b32 v44, 0\n\tv_mov_b32 v45, 0\n\tv_mov_b32 v46, 0\n\tv_mov_b32 v47, 0\n\t"
        "v_mov_b32 v160, 0\n\tv_mov_b32 v161, 0\n\tv_mov_b32 v162, 0\n\tv_mov_b32 v163, 0\n\t"
        "v_mov_b32 v168, %[cnt]\n\tv_mov_b32 v169, 1\n\t"
        WT
        QK("16:31", "128:131")
        "s_mov_b32 s40, 0\n\t"
        "s_mov_b32 s41, 3\n\t"
        "s_nop 10\n\t"
        "1:\n\t"
        TILE4("160:163", "148:151", "152:155", "156:157", "158:159", "160:161", "162:163", "2048")
        TILE4("152:155", "156:159", "160:163", "148:149", "150:151", "152:153", "154:155", "4096")
        "v_add_u32 v165, 0x1000, v165\n\t"
        "s_add_u32 s40, s40, 1\n\t"
        "s_cmp_lt_u32 s40, s41\n\t"
        "s_cbranch_scc1 1b\n\t"
        "s_cmp_eq_u32 s40, 16\n\t"
        "s_cbranch_scc1 4f\n\t"
        "s_cmp_eq_u32 s40, 3\n\t"
        "s_cbranch_scc0 3f\n\t"
        "s_mov_b32 s41, 0\n\t"
        "2:\n\t"
        "ds_read_b32 v170, v168 offset:%[ckv]\n\t"
        "s_add_u32 s41, s41, 1\n\t"
        "s_waitcnt lgkmcnt(0)\n\t"
        "v_readfirstlane_b32 s42, v170\n\t"
        "s_bitcmp1_b32 s41, 22\n\t"
        "s_cselect_b32 s42, 4, s42\n\t"
        "s_cmp_lt_u32 s42, 4\n\t"
        "s_cbranch_scc1 2b\n\t"
        "global_store_dwordx4 %[l16], v[172:175], %[hst] sc1\n\t"
        "global_store_dwordx4 %[l16], v[176:179], %[hst] offset:1024 sc1\n\t"
        "global_store_dwordx4 %[l16], v[180:183], %[hst] offset:2048 sc1\n\t"
        "global_store_dwordx4 %[l16], v[184:187], %[hst] offset:3072 sc1\n\t"
        "s_mov_b32 s41, 5\n\t"
        "s_branch 1b\n\t"
        "3:\n\t"
        "s_waitcnt vmcnt(0)\n\t"
        "s_mov_b64 exec, 1\n\t"
        "ds_add_u32 v168, v169 offset:%[chd]\n\t"
        "v_add_u32 v168, 4, v168\n\t"
        "s_mov_b64 exec, -1\n\t"
        "s_cmp_eq_u32 s40, 5\n\t"
        "s_cselect_b32 s41, 8, 16\n\t"
        "s_branch 1b\n\t"
        "4:\n\t"
        PV("96:111", "160:163", "40:47")
        "s_nop 15\n\t"
        : "+{v[48:63]}"(o[0]), "+{v[64:79]}"(o[1]), "+{v[80:95]}"(o[2]), "+{v[96:111]}"(o[3]), "+{v165}"(ka), "+{v166}"(va)
        : "{v[112:127]}"(cneg), "{v[128:135]}"(q01), "{v[136:143]}"(q23), "{v167}"(vstep), [cnt] "i"(L_CNT), [chd] "i"(4 * C_HDRAIN),
          [ckv] "i"(4 * C_KV2), "{v[172:175]}"(hp[0]), "{v[176:179]}"(hp[1]), "{v[180:183]}"(hp[2]), "{v[184:187]}"(hp[3]), [l16] "v"(l16), [hst] "s"(hst)
        : "memory", "scc", "s40", "s41", "s42", "v16", "v17", "v18", "v19", "v20", "v21", "v22", "v23", "v24", "v25", "v26", "v27", "v28", "v29", "v30",
          "v31", "v32", "v33", "v34", "v35", "v36", "v37", "v38", "v39", "v40", "v41", "v42", "v43", "v44", "v45", "v46", "v47", "v144", "v145",
          "v146", "v147", "v148", "v149", "v150", "v151", "v152", "v153", "v154", "v155", "v156", "v157", "v158", "v159", "v160", "v161", "v162",
          "v163", "v164", "v168", "v169", "v170");
#pragma unroll
    for (int u = 0; u < 4; ++u) mx[u] = bound;
  } else {
#pragma unroll
    for (int i = 0; i < 4; ++i) st16_dev(hst + i * 1024, l16, hp[i]);
    WAITV(0);
    cnt_add<C_HDRAIN>();
    cnt_wait<C_KV2, 4>();
    f32x16 z;
#pragma unroll
    for (int i = 0; i < 16; ++i) z[i] = 0.f;
#pragma unroll
    for (int u = 0; u < 4; ++u) mx[u] = -3.0e38f;
#pragma unroll 1
    for (int t = 0; t < 32; ++t) {
      if (t == 16) cnt_add<C_PROG>();
      const u32x4 kf = lds_r128(ka), v0 = lds_trf(va, 128), v1 = lds_trf(va + 512, 128);
      ka += 2048; va += vstep;
#pragma unroll
      for (int u = 0; u < 4; ++u) {
        f32x16 s = mfma32(kf, qf[u], z);
        float m = s[0];
#pragma unroll
        for (int i = 1; i < 16; ++i) m = fmaxf(m, s[i]);
        m = fmaxf(swap_max(m), mx[u]);
        const float al = __builtin_amdgcn_exp2f(mx[u] - m);
        mx[u] = m;
#pragma unroll
        for (int i = 0; i < 16; ++i) { o[u][i] *= al; s[i] = __builtin_amdgcn_exp2f(s[i] - m); }
        const f32x8 plo = __builtin_shufflevector(s, s, 0, 1, 2, 3, 4, 5, 6, 7), phi = __builtin_shufflevector(s, s, 8, 9, 10, 11, 12, 13, 14, 15);
        o[u] = __builtin_amdgcn_smfmac_f32_32x32x32_bf16(__builtin_bit_cast(bf16x8, v0), __builtin_bit_cast(bf16x16, plo), o[u], 0xdddd, 0, 0);
        o[u] = __builtin_amdgcn_smfmac_f32_32x32x32_bf16(__builtin_bit_cast(bf16x8, v1), __builtin_bit_cast(bf16x16, phi), o[u], 0xdddd, 0, 0);
      }
    }
  }
  TS(30);
  RELANE();
  cnt_wait<C_WO, 4>();
#pragma unroll
  for (int u = 0; u < 2; ++u) {
    const f32x16 v = o[2 + u];
    lds_wf4(8 * U + tg * 4096 + u * 2048 + l16, (f32x4){v[0], v[1], v[2], v[3]});
    lds_wf4(8 * U + tg * 4096 + u * 2048 + 1024 + l16, (f32x4){v[4], v[5], v[6], v[7]});
    lds_wf(L_STAT + tg * 512 + u * 256 + lane * 4, hh ? mx[2 + u] : v[8]);
  }
  TS(31);
  BARRIER();
  TS(32);
  const int mo = qb * 4 + 2 * qg + kh;
#pragma unroll
  for (int u = 0; u < 2; ++u) {
    const unsigned pa = 8 * U + (tg ^ 2) * 4096 + u * 2048 + l16, za = L_STAT + (tg ^ 2) * 512 + u * 256 + r * 4;
    const f32x4 a = lds_rf4(pa), b = lds_rf4(pa + 1024);
    const float lp = lds_rf(za), mp = lds_rf(za + 128);
    const float mn = fmaxf(mx[u], mp);
    const float f0 = __builtin_amdgcn_exp2f(mx[u] - mn), f1 = __builtin_amdgcn_exp2f(mp - mn);
    const float inv = __builtin_amdgcn_rcpf(o[u][8] * f0 + lp * f1);
    const float g0 = f0 * inv, g1 = f1 * inv;
    u32x4 w;
    w[0] = pk_bf16(o[u][0] * g0 + a[0] * g1, o[u][1] * g0 + a[1] * g1);
    w[1] = pk_bf16(o[u][2] * g0 + a[2] * g1, o[u][3] * g0 + a[3] * g1);
    w[2] = pk_bf16(o[u][4] * g0 + b[0] * g1, o[u][5] * g0 + b[1] * g1);
    w[3] = pk_bf16(o[u][6] * g0 + b[2] * g1, o[u][7] * g0 + b[3] * g1);
    st16_dev(ws + WS_O + ((size_t)(mo * 2 + u) * 32 + head) * 1024, l16, w);
  }
  TS(33);
  WAITV(0);
  TS(34);
  st8_dev1(ws + WS_FC, (mo * 32 + head) * 8, tag);
  const size_t eo = ((size_t)(m2 * 64 + (tg >> 1) * 32 + r) * 512 + n2 * 64 + (tg & 1) * 32 + 8 * hh) * 2;
  const u32x4 xv0 = *(const u32x4*)(x + eo), xv1 = *(const u32x4*)(x + eo + 32);
  u32x4 wf[8][2];
#pragma unroll
  for (int s = 0; s < 8; ++s)
#pragma unroll
    for (int b = 0; b < 2; ++b) wf[s][b] = lds_r128(((b ^ (tg & 1)) * 4 * U) + (tg * 8 + s) * 1024 + l16);
  TS(35);
  poll_flags(ws, WS_FC + (m2 * 32 + tg * 8 + (lane & 7)) * 8, tag);
  TS(36);
  const auto rsrc = __builtin_amdgcn_make_buffer_rsrc(ws, 0, WS_END, 0x00020000);
  u32x4 of[8][2];
#pragma unroll
  for (int s = 0; s < 8; ++s)
#pragma unroll
    for (int a = 0; a < 2; ++a)
      of[s][a] = __builtin_amdgcn_raw_buffer_load_b128(rsrc, l16 + (s & 3) * 1024, WS_O + ((m2 * 2 + (a ^ (tg >> 1))) * 32 + tg * 8 + (s & 4)) * 1024, 16);
  SB();
  f32x16 sa[2][2];
#pragma unroll
  for (int a = 0; a < 2; ++a)
#pragma unroll
    for (int b = 0; b < 2; ++b)
#pragma unroll
      for (int i = 0; i < 16; ++i) sa[a][b][i] = 0.f;
#pragma unroll
  for (int s = 0; s < 8; ++s)
#pragma unroll
    for (int a = 0; a < 2; ++a)
#pragma unroll
      for (int b = 0; b < 2; ++b) sa[a][b] = mfma32(wf[s][b], of[s][a], sa[a][b]);
  asm volatile("" : "+v"(sa[0][0]), "+v"(sa[0][1]), "+v"(sa[1][0]), "+v"(sa[1][1]));
  TS(37);
  auto aslot = [&](int src, int d) LAM { return (d == 3 ? 4 * U : (d - 1) * 4096) + src * 8192 + l16; };
#pragma unroll
  for (int d = 1; d < 4; ++d) {
    const f32x16 v = sa[d >> 1][d & 1];
#pragma unroll
    for (int j = 0; j < 4; ++j) lds_wf4(aslot(tg, d) + j * 1024, (f32x4){v[4 * j], v[4 * j + 1], v[4 * j + 2], v[4 * j + 3]});
  }
  cnt_add<C_AX>();
  TS(38);
  cnt_wait<C_AX, 4>();
  TS(39);
  f32x16 sv = sa[0][0];
#pragma unroll
  for (int d = 1; d < 4; ++d)
#pragma unroll
    for (int j = 0; j < 4; ++j) {
      const f32x4 v = lds_rf4(aslot(tg ^ d, d) + j * 1024);
#pragma unroll
      for (int i = 0; i < 4; ++i) sv[4 * j + i] += v[i];
    }
  TS(40);
  cnt_wait<C_G, 4>();
  TS(41);
  u32x4 res[2];
#pragma unroll
  for (int j = 0; j < 4; ++j) {
    const f32x4 gv = lds_rf4(7 * U + tg * 4096 + j * 1024 + l16);
#pragma unroll
    for (int i = 0; i < 2; ++i) {
      const unsigned xw = j < 2 ? xv0[2 * j + i] : xv1[2 * (j - 2) + i];
      res[j >> 1][2 * (j & 1) + i] = pk_bf16(u2f(xw << 16) + sv[4 * j + 2 * i] * gv[2 * i],
                                             u2f(xw & 0xffff0000u) + sv[4 * j + 2 * i + 1] * gv[2 * i + 1]);
    }
  }
  *(u32x4*)(out + eo) = res[0];
  *(u32x4*)(out + eo + 32) = res[1];
  if (blockIdx.x == 0 && wid == 0) st8_dev1(ws, 0u, tag);
  TS(42);
  TSFLUSH();
  asm volatile("s_endpgm\n\t" TILE_SUB_TEXT :: "i"(L_CNT + 4 * C_DRAIN) : "memory");
}

extern "C" void kernel_launch(void* const* d_in, const int* in_sizes, int n_in, void* d_out, int out_size, void* d_ws, size_t ws_size,
                              hipStream_t stream) {
  (void)hipFuncSetAttribute((const void*)fused_kernel, hipFuncAttributeMaxDynamicSharedMemorySize, L_TOTAL);
  fused_kernel<<<256, 512, L_TOTAL, stream>>>((const char*)d_in[0], (const char*)d_in[1], (const char*)d_in[2], (const char*)d_in[4],
                                              (const char*)d_in[5], (const char*)d_in[6], (const char*)d_in[7], (char*)d_out, (char*)d_ws);
}
```

```cpp
#include <hip/hip_runtime.h>
#include <hip/hip_bf16.h>

typedef __attribute__((ext_vector_type(8))) __bf16 bf16x8;
typedef __attribute__((ext_vector_type(16))) __bf16 bf16x16;
typedef __attribute__((ext_vector_type(2))) __bf16 bf16x2;
typedef __attribute__((ext_vector_type(16))) float f32x16;
typedef __attribute__((ext_vector_type(8))) float f32x8;
typedef __attribute__((ext_vector_type(4))) float f32x4;
typedef __attribute__((ext_vector_type(2))) float f32x2;
typedef __attribute__((ext_vector_type(4))) unsigned u32x4;
typedef __attribute__((ext_vector_type(8))) unsigned u32x8;
typedef __attribute__((ext_vector_type(32))) unsigned u32x32;
typedef __attribute__((ext_vector_type(2))) unsigned u32x2;
typedef __attribute__((ext_vector_type(4))) short s16x4;
typedef unsigned long long u64;
#define LDS_AS __attribute__((address_space(3)))
#define GLB_AS __attribute__((address_space(1)))
#define DI __device__ __forceinline__

constexpr unsigned WS_FLAG = 0;
constexpr unsigned WS_FA = 1024;
constexpr unsigned WS_FB = 4096;
constexpr unsigned WS_FC = 8192;
constexpr unsigned WS_KMAX = 16384;
constexpr unsigned WS_Q = 32768;
constexpr unsigned WS_KV = WS_Q + (2u << 20);
constexpr unsigned WS_H = WS_KV + (4u << 20);
constexpr unsigned WS_W2 = WS_H + (7u << 20);
constexpr unsigned WS_WO = WS_W2 + 1792u * 1024u;
constexpr unsigned WS_O = WS_WO + 512u * 1024u;
constexpr unsigned WS_END = WS_O + (2u << 20);

constexpr unsigned U = 16384;
constexpr unsigned L_MISC = 9 * U, L_AUG = L_MISC, L_STAT = L_MISC + 4096, L_CNT = L_MISC + 6144, L_CONST = L_CNT + 128,
                   L_QF = L_MISC + 7168, L_KM = L_QF + 8192, L_TOTAL = 10 * U;
enum { C_DRAIN = 0, C_CS, C_KV1, C_KV2, C_HDRAIN, C_PROG  , C_WO, C_FX, C_G, C_AX, C_FB };
constexpr float BOUND_LIMIT = 40.f;
#define NBG 8

#define TS(i)
#define TSFLUSH()

#define f2u(x) __builtin_bit_cast(unsigned, (float)(x))
#define u2f(x) __builtin_bit_cast(float, (unsigned)(x))
#define fmaxf __builtin_fmaxf
DI f32x16 mfma32(u32x4 a, u32x4 b, f32x16 c) {
  return __builtin_amdgcn_mfma_f32_32x32x16_bf16(__builtin_bit_cast(bf16x8, a), __builtin_bit_cast(bf16x8, b), c, 0, 0, 0);
}
DI f32x4 fragsum(u32x4 sel, int selidx, u32x4 f0, u32x4 f1, f32x4 c) {
  const u32x8 b = __builtin_shufflevector(f0, f1, 0, 1, 2, 3, 4, 5, 6, 7);
  return __builtin_amdgcn_smfmac_f32_16x16x64_bf16(__builtin_bit_cast(bf16x8, sel), __builtin_bit_cast(bf16x16, b), c, selidx, 0, 0);
}
DI unsigned pk_bf16(float lo, float hi) {
  f32x2 f = {lo, hi};
  return __builtin_bit_cast(unsigned, __builtin_convertvector(f, bf16x2));
}
DI float rbf(float x) { return (float)(__bf16)x; }
DI float dot2(unsigned a, unsigned b, float c) {
  return __builtin_amdgcn_fdot2_f32_bf16(__builtin_bit_cast(bf16x2, a), __builtin_bit_cast(bf16x2, b), c, false);
}
DI float sq8(u32x4 v) { return dot2(v[0], v[0], dot2(v[1], v[1], dot2(v[2], v[2], dot2(v[3], v[3], 0.f)))); }
DI u32x4 lds_r128(unsigned addr) { return *(const LDS_AS u32x4*)(uintptr_t)addr; }
DI f32x4 lds_rf4(unsigned addr) { return *(const LDS_AS f32x4*)(uintptr_t)addr; }
DI float lds_rf(unsigned addr) { return *(const LDS_AS float*)(uintptr_t)addr; }
DI void lds_wu(unsigned addr, unsigned v) { *(LDS_AS unsigned*)(uintptr_t)addr = v; }
DI void lds_wf(unsigned addr, float v) { *(LDS_AS float*)(uintptr_t)addr = v; }
DI void lds_wf2(unsigned addr, f32x2 v) { *(LDS_AS f32x2*)(uintptr_t)addr = v; }
DI void lds_wf4(unsigned addr, f32x4 v) { *(LDS_AS f32x4*)(uintptr_t)addr = v; }
DI void lds_w128(unsigned addr, u32x4 v) { *(LDS_AS u32x4*)(uintptr_t)addr = v; }
DI u32x2 lds_tr(unsigned addr) {
  return __builtin_bit_cast(u32x2, __builtin_amdgcn_ds_read_tr16_b64_v4i16((LDS_AS s16x4*)(uintptr_t)addr));
}
DI u32x4 lds_trf(unsigned addr, unsigned step) {
  const u32x2 lo = lds_tr(addr), hi = lds_tr(addr + step);
  return (u32x4){lo[0], lo[1], hi[0], hi[1]};
}
DI float swap_add(float v) {
  auto r = __builtin_amdgcn_permlane32_swap(f2u(v), f2u(v), false, false);
  return u2f(r[0]) + u2f(r[1]);
}
DI float swap_max(float v) {
  auto r = __builtin_amdgcn_permlane32_swap(f2u(v), f2u(v), false, false);
  return fmaxf(u2f(r[0]), u2f(r[1]));
}
DI unsigned umax(unsigned a, unsigned b) { return a > b ? a : b; }
DI unsigned umax32(unsigned m) {
  m = umax(m, (unsigned)__builtin_amdgcn_update_dpp(0, (int)m, 0xB1, 0xf, 0xf, true));
  m = umax(m, (unsigned)__builtin_amdgcn_update_dpp(0, (int)m, 0x4E, 0xf, 0xf, true));
  m = umax(m, (unsigned)__builtin_amdgcn_update_dpp(0, (int)m, 0x141, 0xf, 0xf, true));
  m = umax(m, (unsigned)__builtin_amdgcn_update_dpp(0, (int)m, 0x140, 0xf, 0xf, true));
  auto r = __builtin_amdgcn_permlane16_swap(m, m, false, false);
  return umax(r[0], r[1]);
}
DI void glds(const void* base, unsigned voff, unsigned lds) {
  asm volatile("s_mov_b32 m0, %2\n\ts_nop 0\n\tglobal_load_lds_dwordx4 %0, %1" ::"v"(voff), "s"(base), "s"(lds) : "memory");
}
DI void glds4_dev(const void* base, unsigned voff, unsigned lds) {
  asm volatile("s_mov_b32 m0, %2\n\ts_nop 0\n\tglobal_load_lds_dwordx4 %0, %1 sc1\n\tglobal_load_lds_dwordx4 %0, %1 offset:1024 sc1\n\t"
               "global_load_lds_dwordx4 %0, %1 offset:2048 sc1\n\tglobal_load_lds_dwordx4 %0, %1 offset:3072 sc1" ::"v"(voff), "s"(base), "s"(lds) : "memory");
}
DI void glds2_dev(const void* base, unsigned voff, unsigned lds) {
  asm volatile("s_mov_b32 m0, %2\n\ts_nop 0\n\tglobal_load_lds_dwordx4 %0, %1 sc1\n\tglobal_load_lds_dwordx4 %0, %1 offset:1024 sc1" ::"v"(voff), "s"(base), "s"(lds) : "memory");
}
DI void st16_dev(void* base, unsigned voff, u32x4 v) {
  asm volatile("global_store_dwordx4 %0, %1, %2 sc1\n\ts_nop 1" ::"v"(voff), "v"(v), "s"(base) : "memory");
}
DI void st4_dev2(void* base, unsigned voff, unsigned v) {
  asm volatile("s_mov_b64 exec, %3\n\tglobal_store_dword %0, %1, %2 sc1\n\ts_mov_b64 exec, -1" ::"v"(voff), "v"(v), "s"(base), "s"(0x100000001ull) : "memory");
}
DI void st8_dev1(void* base, unsigned voff, u64 v) {
  asm volatile("s_mov_b64 exec, 1\n\tglobal_store_dwordx2 %0, %1, %2 sc1\n\ts_mov_b64 exec, -1" ::"v"(voff), "v"(v), "s"(base) : "memory");
}
DI unsigned pin(unsigned v) { asm volatile("" : "+v"(v)); return v; }
#define BARRIER() asm volatile("s_waitcnt lgkmcnt(0)\n\ts_barrier" ::: "memory")
#define WAITV(n) asm volatile("s_waitcnt vmcnt(" #n ")" ::: "memory")
#define WAITL() asm volatile("s_waitcnt lgkmcnt(0)" ::: "memory")
#define SB() __builtin_amdgcn_sched_barrier(0)
template <int C> DI void cnt_add() {
  asm volatile("s_waitcnt lgkmcnt(0)\n\ts_mov_b64 exec, 1\n\tds_add_u32 %0, %1 offset:%c2\n\ts_mov_b64 exec, -1" ::"v"(L_CNT), "v"(1u), "i"(4 * C) : "memory");
}
template <int C, int N, int SLEEP = 1> DI void cnt_wait() {
  unsigned t, i, v;
  asm volatile("s_mov_b32 %1, 0\n\ts_branch 2f\n\t1:\n\ts_sleep %c6\n\t2:\n\tds_read_b32 %0, %3 offset:%c4\n\ts_add_u32 %1, %1, 1\n\ts_waitcnt lgkmcnt(0)\n\t"
               "v_readfirstlane_b32 %2, %0\n\ts_bitcmp1_b32 %1, 20\n\ts_cselect_b32 %2, %c5, %2\n\ts_cmp_lt_u32 %2, %c5\n\ts_cbranch_scc1 1b"
               : "=&v"(t), "=&s"(i), "=&s"(v) : "v"(L_CNT), "i"(4 * C), "i"(N), "i"(SLEEP) : "memory", "scc");
}
template <int SLEEP = 2> DI void poll_flags(const char* ws, unsigned voff, u64 tag) {
  u64 v; unsigned i;
  asm volatile("s_mov_b32 %1, 0\n\ts_branch 2f\n\t1:\n\ts_sleep %c5\n\t2:\n\tglobal_load_dwordx2 %0, %2, %3 sc1\n\ts_add_u32 %1, %1, 1\n\ts_waitcnt vmcnt(0)\n\t"
               "v_cmp_ne_u64 vcc, %4, %0\n\ts_bitcmp1_b32 %1, 17\n\ts_cbranch_scc1 3f\n\ts_cbranch_vccnz 1b\n\t3:"
               : "=&v"(v), "=&s"(i) : "v"(voff), "s"(ws), "s"(tag), "i"(SLEEP) : "memory", "scc", "vcc");
}

#define CLOB_W "v56", "v57", "v58", "v59", "v60", "v61", "v62", "v63", "v64", "v65", "v66", "v67", "v68", "v69", "v70", "v71", "v72", "v73", "v74", "v75", \
               "v76", "v77", "v78", "v79", "v80", "v81", "v82", "v83", "v84", "v85", "v86", "v87", "v88", "v89", "v90", "v91", "v92", "v93", "v94", "v95"
#define P1_DX(J) "s_add_u32 m0, s57, (t&3)*16384+1024*" #J "\n\ts_nop 0\n\tglobal_load_lds_dwordx4 v[34+" #J "], s[48:49]\n\t"
#define P1_DX1 P1_DX(1) "s_add_u32 s48, s48, 128\n\ts_addc_u32 s49, s49, 0\n\t"
#define P1_DW "s_add_u32 m0, s56, 65536+(t&3)*8192\n\ts_nop 0\n\tglobal_load_lds_dwordx4 v36, s[50:51]\n\ts_add_u32 s50, s50, 64*3072\n\ts_addc_u32 s51, s51, 0\n\t"
#define P1_DQ ".if (t&1)==0\n\ts_add_u32 m0, s56, 98304+(t/2)*8192\n\ts_nop 0\n\tglobal_load_lds_dwordx4 v37, s[52:53]\n\ts_add_u32 s52, s52, 128*3072\n\ts_addc_u32 s53, s53, 0\n\t.endif\n\t"
#define P1_DF(J) "s_add_u32 m0, s57, (t&3)*16384+1024*" #J "\n\ts_nop 0\n\tglobal_load_lds_dwordx4 v[38+" #J "], s[54:55]\n\t"
#define P1_DF1 P1_DF(1) "s_add_u32 s54, s54, 128*3584\n\ts_addc_u32 s55, s55, 0\n\t"
#define P1_D0 ".if t<8\n\t" P1_DX(0) ".elseif t<11\n\t" P1_DF(0) ".endif\n\t"
#define P1_D1 ".if t<8\n\t" P1_DX1 ".elseif t<11\n\t" P1_DF1 ".endif\n\t"
#define P1_D2 ".if t<8\n\t" P1_DW ".endif\n\t"
#define P1_D3 ".if t<8\n\t" P1_DQ ".endif\n\t"
#define P1_WR(I, Z) ".if " #I "==0\n\t.set w, 64+4*" Z "\n\t.else\n\t.set w, 68+12*" Z "+4*" #I "\n\t.endif\n\t"
#define P1_RD(I) ".if s<8\n\t" P1_WR(I, "(s&1)") "ds_read_b128 v[128+16*s+4*" #I ":131+16*s+4*" #I "], v[24+" #I "] offset:(s&3)*16384\n\t" \
                 "ds_read_b64_tr_b16 v[w:w+1], v[28+" #I "] offset:(s&3)*8192\n\tds_read_b64_tr_b16 v[w+2:w+3], v[28+" #I "] offset:(s&3)*8192+512\n\t.endif\n\t"
#define P1_RQ(I) ".if s<8\n\t.set w, 48+8*" #I "+4*(s&1)\n\tds_read_b64_tr_b16 v[w:w+1], v[32+" #I "] offset:s*4096\n\tds_read_b64_tr_b16 v[w+2:w+3], v[32+" #I "] offset:s*4096+256\n\t.endif\n\t"
#define P1_MM(I) ".if s>0\n\t" P1_WR(I, "(p&1)") ".set xr, 128+16*p+4*" #I "\n\t.if (p==0)&&(" #I "==1)\n\tv_mfma_f32_32x32x16_bf16 v[96:111], v[w:w+3], v[xr:xr+3], 0\n\t.else\n\t" \
                 "v_mfma_f32_32x32x16_bf16 v[96:111], v[w:w+3], v[xr:xr+3], v[96:111]\n\t.endif\n\t.endif\n\t"
#define P1_MQ(I) ".if s>0\n\t.set w, 48+8*" #I "+4*(p&1)\n\t.set xr, 128+16*p+4*" #I "\n\t.if (p+" #I ")==0\n\tv_mfma_f32_32x32x16_bf16 v[112:127], v[w:w+3], v[xr:xr+3], 0\n\t.else\n\t" \
                 "v_mfma_f32_32x32x16_bf16 v[112:127], v[w:w+3], v[xr:xr+3], v[112:127]\n\t.endif\n\t.endif\n\t"
#define P1_TS(B)
#define P1_TSOP
#define P1_TSCL
#define P1_SQ(J) ".if s>0\n\t.set xr, 128+16*p+" #J "\n\tv_and_b32 v4, v8, v[xr]\n\tv_lshlrev_b32 v5, 16, v[xr]\n\tv_fmac_f32 v45, v4, v4\n\tv_fmac_f32 v46, v5, v5\n\t.endif\n\t"
#define P1_HEAD_TEXT \
  "s_setprio 3\n\t.set t, 0\n\t" P1_D0 P1_D1 P1_D2 P1_D3 "s_setprio 2\n\t.set t, 1\n\t" P1_D0 P1_D1 P1_D2 P1_D3 "s_setprio 0"
#define P1_TEXT \
  "v_mov_b32 v8, 0xffff0000\n\t.set s, 0\n\t.rept 9\n\t.set p, s-1\n\t.set t, s+3\n\t" P1_TS("44") \
  ".if s==0\n\ts_waitcnt vmcnt(6) lgkmcnt(0)\n\t.elseif s==1\n\ts_waitcnt vmcnt(10) lgkmcnt(0)\n\t.elseif s<6\n\ts_waitcnt vmcnt(7) lgkmcnt(0)\n\t.elseif s==6\n\ts_waitcnt vmcnt(5) lgkmcnt(0)\n\t" \
  ".elseif s==7\n\ts_waitcnt vmcnt(4) lgkmcnt(0)\n\t.else\n\ts_waitcnt lgkmcnt(0)\n\t.endif\n\t.if s<8\n\ts_barrier\n\t.endif\n\t" P1_TS("54") \
  ".if s==0\n\t" P1_RD(0) P1_RD(1) P1_RD(2) P1_RD(3) P1_RQ(0) P1_RQ(1) ".set t, 2\n\t" P1_D0 P1_D1 P1_D2 P1_D3 ".set t, 3\n\t" P1_D0 P1_D1 P1_D2 P1_D3 ".else\n\t" \
  P1_MM(1) P1_D0 P1_RD(1) P1_SQ(0) P1_MM(2) P1_D1 P1_RD(2) P1_SQ(1) \
  ".if (s&1)==0\n\tv_smfmac_f32_16x16x64_bf16 v[12:15], v[40:43], v[64:71], v44\n\tv_smfmac_f32_16x16x64_bf16 v[16:19], v[40:43], v[48:55], v44\n\t.endif\n\t" \
  P1_MM(3) P1_D2 P1_RD(3) P1_SQ(2) P1_MM(0) P1_D3 P1_RD(0) P1_SQ(3) P1_MQ(0) P1_RQ(0) P1_RQ(1) P1_SQ(4) P1_SQ(5) P1_MQ(1) P1_SQ(6) \
  "v_smfmac_f32_16x16x64_bf16 v[20:23], v[40:43], v[128+16*p:135+16*p], v44\n\t" P1_SQ(7) \
  ".endif\n\t.set s, s+1\n\t.endr\n\ts_nop 7"
#define TL_W(N) ".if (" N "&3)==0\n\t.set w, 56+((" N "/4)&3)*4\n\t.else\n\t.set w, 72+(((" N "/4)*3+(" N "&3)-1)%%6)*4\n\t.endif\n\t"
#define TL_RD ".set n, f+5\n\t.if n<64\n\t.set o, ((n/4)&7)*8192\n\t.set b, 24+(n/32)*4+(n&3)\n\t" TL_W("n") \
              "ds_read_b64_tr_b16 v[w:w+1], v[b] offset:o\n\tds_read_b64_tr_b16 v[w+2:w+3], v[b] offset:o+512\n\t.endif\n\t"
#define TL_MM(A, C) ".if f<59\n\ts_waitcnt lgkmcnt(10)\n\t.else\n\ts_waitcnt lgkmcnt(2*(63-f))\n\t.endif\n\t" TL_W("f") ".set x, 128+(f&31)*4\n\t" \
              ".if (f&31)==0\n\tv_mfma_f32_32x32x16_bf16 v[" A "], v[w:w+3], v[x:x+3], 0\n\t.else\n\tv_mfma_f32_32x32x16_bf16 v[" A "], v[w:w+3], v[x:x+3], v[" A "]\n\t.endif\n\t" \
              ".if (f&7)==4\n\tv_smfmac_f32_16x16x64_bf16 v[" C "], v[40:43], v[w-4:w+3], v44\n\t.endif\n\t"
#define TILE_SUB_TEXT \
  ".Ltile_sub:\n\t.set f, -5\n\t.rept 5\n\t" TL_RD ".set f, f+1\n\t.endr\n\t" \
  ".set k, 16\n\t.rept 8\n\tv_mov_b32 v[k], 0\n\t.set k, k+1\n\t.endr\n\t" \
  ".rept 32\n\t" TL_RD TL_MM("96:111", "16:19") ".set f, f+1\n\t.endr\n\t" \
  "s_cmp_eq_u32 s46, 0\n\ts_cbranch_scc1 .Ltile_ret\n\ts_cmp_eq_u32 s46, 1\n\ts_cbranch_scc1 .Ltile_two\n\t" \
  "v_mov_b32 v45, %c0\n\tv_mov_b32 v46, 1\n\ts_waitcnt vmcnt(0)\n\ts_mov_b64 exec, 1\n\tds_add_u32 v45, v46\n\ts_mov_b64 exec, -1\n\t" \
  ".Ltile_two:\n\t.rept 32\n\t" TL_RD TL_MM("112:127", "20:23") ".set f, f+1\n\t.endr\n\t" \
  ".Ltile_ret:\n\ts_waitcnt lgkmcnt(0)\n\ts_nop 9\n\ts_setpc_b64 s[44:45]\n\t"
#define TILES(BASES, MODE)   \
  asm volatile("s_cmp_eq_u32 s46, 3\n\ts_cbranch_scc1 1f\n\ts_call_b64 s[44:45], .Ltile_sub\n\t1:" : "+{v[96:111]}"(accA), "+{v[112:127]}"(accB), "={v[16:23]}"(cst) \
               : "{v[128:159]}"(X0), "{v[160:191]}"(X1), "{v[192:223]}"(X2), "{v[224:255]}"(X3), "{v[24:31]}"(BASES), "{v[40:43]}"(sel), "{v44}"(selidx), \
                 "{s46}"(MODE) : "memory", "scc", "s44", "s45", "v45", "v46", CLOB_W)

#define E(r) "v_exp_f32 v" #r ", v" #r "\n\t"
#define QK(Dst, Q) "v_mfma_f32_32x32x16_bf16 v[" Dst "], v[144:147], v[" Q "], v[112:127]\n\t"
#define PV(O, V, P) "v_smfmac_f32_32x32x32_bf16 v[" O "], v[" V "], v[" P "], v164\n\t"
#define UA(OP, VP1, QN, O, V0, X0, X1) E(16) E(17) PV(OP, VP1, "40:47") E(18) E(19) E(20) X0 QK("32:47", QN) X1 E(21) E(22) E(23) E(24) E(25) E(26) PV(O, V0, "16:23") E(27) E(28) E(29) E(30) E(31)
#define UB(OP, VP1, QN, O, V0, X0, X1) E(32) E(33) PV(OP, VP1, "24:31") E(34) E(35) E(36) X0 QK("16:31", QN) X1 E(37) E(38) E(39) E(40) E(41) E(42) PV(O, V0, "32:39") E(43) E(44) E(45) E(46) E(47)
#define TR(D, OFF) "ds_read_b64_tr_b16 v[" D "], v166 offset:" OFF "\n\t"
#define KRD(OFF) "ds_read_b128 v[144:147], v165 offset:" OFF "\n\t"
#define WT "s_waitcnt lgkmcnt(0)\n\t"
#define VADV "v_add_u32 v166, v167, v166\n\t"
#define TILE4(VP1, V0, V1, N0, N1, N2, N3, OK)                                          \
  UA("96:111", VP1, "132:135", "48:63", V0, VADV, TR(N0, "0") TR(N1, "128"))            \
  UB("48:63", V1, "136:139", "64:79", V0, "", TR(N2, "512") TR(N3, "640"))              \
  UA("64:79", V1, "140:143", "80:95", V0, "", KRD(OK))                                  \
  UB("80:95", V1, "128:131", "96:111", V0, WT, "")

#if defined(__HIP_DEVICE_COMPILE__)
#define NOPK __attribute__((target("no-packed-fp32-ops")))
#else
#define NOPK
#endif
#define LAM NOPK __attribute__((always_inline))
__global__ NOPK __launch_bounds__(512, 2) void fused_kernel(const char* __restrict__ x, const char* __restrict__ wqkv, const char* __restrict__ wo,
                                                       const char* __restrict__ w1, const char* __restrict__ b1, const char* __restrict__ w2,
                                                       const char* __restrict__ b2, char* __restrict__ out, char* __restrict__ ws) {
  extern __shared__ __attribute__((aligned(16))) char smem[];
  const int wid = __builtin_amdgcn_readfirstlane(threadIdx.x >> 6);
  const int xcd = blockIdx.x & 7, idx = blockIdx.x >> 3;
  const int g = wid >> 2, tg = wid & 3;
  int lane, r, hh, ch, q4, p4, p4s;
  unsigned l16;
#define RELANE() do { asm volatile("v_mbcnt_lo_u32_b32 %0, -1, 0\n\tv_mbcnt_hi_u32_b32 %0, -1, %0" : "=v"(lane)); __builtin_assume(lane >= 0 && lane < 64); \
    r = lane & 31; hh = lane >> 5; ch = (lane >> 4) & 1; q4 = (lane & 15) >> 2; p4 = lane & 3; l16 = lane * 16; \
    p4s = ((p4 & 1) << 1) | (p4 >> 1);   } while (0)
  RELANE();
  const int rs = (r & 19) | ((r & 4) << 1) | ((r & 8) >> 1);
  TS(0);
  const int mt = (xcd >> 1) * 4 + (idx & 3), np = (xcd & 1) * 8 + (idx >> 2);
  const bool isk = np < 8, even = !(np & 1);
  const int f01 = even ? 2 * np : 32 + (np & ~1), f2 = even ? 2 * np + 2 : 48 + (np >> 1);
  const unsigned vp = ((lane & 7) ^ ((lane >> 2) & 4)) << 4, vf = (lane >> 3) * 3584 + vp;
  u64 sx = (u64)(x + (size_t)(mt * 128 + 16 * wid) * 1024), sw = (u64)(wqkv + 1024 + np * 128 + (size_t)(8 * wid) * 3072),
      sq = (u64)(wqkv + np * 64 + (size_t)(16 * wid) * 3072), sf = (u64)(w1 + f01 * 64 + (size_t)(16 * wid) * 3584);
  const unsigned vx0 = (lane >> 3) * 1024 + (((lane & 7) ^ (lane >> 4)) << 4);
  const u32x2 vx = {vx0, (vx0 ^ 64) + 8192}, vwq = {(lane >> 3) * 3072 + vp, (lane >> 2) * 3072u + (lane & 3) * 16}, vff = {vf, vf + 8 * 3584};
  asm volatile(P1_HEAD_TEXT : "+{s[48:49]}"(sx), "+{s[50:51]}"(sw), "+{s[52:53]}"(sq)
               : "{v[34:35]}"(vx), "{v[36:37]}"(vwq), "{s56}"(wid * 1024), "{s57}"(wid * 2048) : "memory", "scc");
  unsigned bias0, bias1, pf_junk;
  {
    const unsigned bo = ((g ? f2 : f01) * 32 + rs) * 2, bo1 = (g && !even) ? bo : bo + 64;
    u64 pc;
    unsigned len;
    asm volatile("s_getpc_b64 %0\n\t.Lpf_base:\n\ts_mov_b32 %1, .Ltile_ret-.Lpf_base" : "=s"(pc), "=s"(len));
    const u64 p01 = (tg & 1) ? (u64)wqkv : (u64)x, p23 = (tg & 1) ? pc : (u64)w1, pb = (tg & 2) ? p23 : p01;
    const unsigned n01 = (tg & 1) ? 6144u : 8192u, n23 = (tg & 1) ? 8192u : 7168u, nb = (tg & 2) ? n23 : n01;
    const unsigned o0 = (tg == 3 ? (idx & 3) : blockIdx.x) * nb, lim = tg == 3 ? len : o0 + nb - 128;
    const unsigned co = o0 + (g ? lane * 128 : 0);
    asm volatile("global_load_ubyte %2, %5, %6\n\tglobal_load_ushort %0, %3, %7\n\tglobal_load_ushort %1, %4, %7"
                 : "=&{v10}"(bias0), "=&{v11}"(bias1), "=&{v47}"(pf_junk) : "v"(bo), "v"(bo1), "v"(co < lim ? co : lim), "s"(pb), "s"(b1) : "memory");
  }
  u64 epoch;
  asm volatile("s_load_dwordx2 %0, %1, 0x0" : "=s"(epoch) : "s"(ws) : "memory");
  if (wid == 4) {
    lds_wu(L_CNT + r * 4, 0u);
    lds_w128(L_CONST + (lane < 48 ? lane : 47) * 16, (u32x4){0x3f80u, 0u, 0u, 0u});
  }
  const unsigned ones = 0x3f803f80u;
  const bool selon = (lane & 3) < 2, selhi = (lane >> 2) & 1;
  const u32x4 sel = {selon && !selhi ? ones : 0u, selon && !selhi ? ones : 0u, selon && selhi ? ones : 0u, selon && selhi ? ones : 0u};
  const int selidx = (lane & 3) == 0 ? 0x4444 : 0xEEEE;
  const unsigned wl = (8 * hh + q4) * 64 + ch * 32 + p4s * 8;
  const unsigned wp = (8 * hh + q4) * 128 + (q4 >> 1) * 64 + ch * 32 + p4s * 8;
  const int rot = tg ^ (2 * g);
  u32x4 wa;
  u32x32 X0, X1, X2, X3;
  f32x16 accA, accB;
  auto put_cs = [&](float c, int aug, unsigned bias) LAM {
    const float chi = rbf(c);
    const unsigned cw = pin(pk_bf16(chi, c - chi)), bw = pin(tg == 0 ? bias : 0u);
    lds_wu(L_AUG + aug * 1024 + l16 + tg * 4, hh ? bw : cw);
  };
  {
    f32x4 cs = {0.f, 0.f, 0.f, 0.f}, cq = cs, xs = cs;
    float xq0 = 0.f, xq1 = 0.f;
    const int row = 32 * tg + r;
    u32x4 xa;
#pragma unroll
    for (int i = 0; i < 4; ++i) {
      xa[i] = row * 128 + (((2 * (i ^ rot) + hh) ^ ((row >> 1) & 7)) << 4);
      wa[i] = pin(((wp + (i ^ rot) * 2048) ^ (g * 64)) + 4 * U);
    }
    const u32x2 qa = {(wl ^ (g * 32)) + rot * 1024 + 6 * U, (wl ^ (g * 32)) + (rot ^ 1) * 1024 + 6 * U};
    TS(1);
    asm volatile(P1_TEXT
                 : "={v[128:159]}"(X0), "={v[160:191]}"(X1), "={v[192:223]}"(X2), "={v[224:255]}"(X3), "={v[96:111]}"(accA), "={v[112:127]}"(accB),
                   "+{v[12:15]}"(cs), "+{v[16:19]}"(cq), "+{v[20:23]}"(xs), "+{v45}"(xq0), "+{v46}"(xq1), "+{s[48:49]}"(sx), "+{s[50:51]}"(sw), "+{s[52:53]}"(sq),
                   "+{s[54:55]}"(sf), "+s"(epoch), "+{v10}"(bias0), "+{v11}"(bias1), "+{v47}"(pf_junk) P1_TSOP
                 : "{v[24:27]}"(xa), "{v[28:31]}"(wa), "{v[32:33]}"(qa), "{v[34:35]}"(vx), "{v[36:37]}"(vwq), "{v[38:39]}"(vff), "{s56}"(wid * 1024),
                   "{s57}"(wid * 2048), "{v[40:43]}"(sel), "{v44}"(selidx)
                 : "memory", "scc", "v4", "v5", "v8", "v48", "v49", "v50", "v51", "v52", "v53", "v54", "v55", CLOB_W P1_TSCL);
    TS(2);
    RELANE();
    lds_wf4(8 * U + wid * 2048 + l16, (f32x4){accB[8], accB[9], accB[10], accB[11]});
    lds_wf4(8 * U + wid * 2048 + 1024 + l16, (f32x4){accB[12], accB[13], accB[14], accB[15]});
    lds_wf2(L_STAT + (32 * tg + r) * 16 + g * 8, (f32x2){xs[0] + xs[1], swap_add(xq0 + xq1)});
    put_cs(cs[0] + cs[1], g, 0u);
    put_cs(cq[0] + cq[1], 2 + g, 0u);
  }
  BARRIER();
  TS(3);
  float rstd;
  u32x4 xaug;
  {
    const f32x4 st = lds_rf4(L_STAT + (32 * tg + r) * 16);
    const float mean = (st[0] + st[2]) * (1.f / 512.f);
    const float var = fmaxf((st[1] + st[3]) * (1.f / 512.f) - mean * mean, 0.f);
    const float sd = rbf(__builtin_amdgcn_sqrtf(rbf(rbf(var) + 1e-5f)));
    rstd = __builtin_amdgcn_rcpf(sd);
    const unsigned nm = pin(pk_bf16(-mean, -mean)), sw_ = pin(pk_bf16(sd, 0.f)), nz = hh ? 0u : nm;
    xaug = (u32x4){hh ? sw_ : nm, nz, nz, nz};
  }
  glds((const void*)sf, vff[0], 3 * U + wid * 2048);
  glds((const void*)sf, vff[1], 3 * U + wid * 2048 + 1024);
  const int tt = mt * 4 + tg;
  auto finish = [&](f32x16 a, int aug, bool relu2, u32x4& o0, u32x4& o1) LAM {
    a = mfma32(lds_r128(L_AUG + aug * 1024 + l16), xaug, a);
#pragma unroll
    for (int i = 0; i < 8; ++i) {
      float u = a[2 * i] * rstd, v = a[2 * i + 1] * rstd;
      if (relu2) { u = fmaxf(u, 0.f); v = fmaxf(v, 0.f); u *= u; v *= v; }
      if (i < 4) o0[i] = pk_bf16(u, v); else o1[i - 4] = pk_bf16(u, v);
    }
  };
  {
    u32x4 o0, o1, oq;
    const f32x16 a = mfma32(lds_r128(L_AUG + (2 + g) * 1024 + l16), xaug, accB);
    const f32x4 r0 = lds_rf4(8 * U + (wid ^ 4) * 2048 + l16), r1 = lds_rf4(8 * U + (wid ^ 4) * 2048 + 1024 + l16);
    finish(accA, g, false, o0, o1);
    const int head = (np & 7) * 4 + g * 2;
    char* kv = ws + WS_KV + ((size_t)head * 64 + tt) * 2048;
    const unsigned vo = isk ? l16 : 1024 + r * 32 + hh * 16;
    st16_dev(kv, vo, o0);
    st16_dev(kv + 131072, vo, o1);
    const float fq = rstd * (0.25f * 1.4426950408889634f);
    oq[0] = pk_bf16((a[0] + r0[0]) * fq, (a[1] + r0[1]) * fq);
    oq[1] = pk_bf16((a[2] + r0[2]) * fq, (a[3] + r0[3]) * fq);
    oq[2] = pk_bf16((a[4] + r1[0]) * fq, (a[5] + r1[1]) * fq);
    oq[3] = pk_bf16((a[6] + r1[2]) * fq, (a[7] + r1[3]) * fq);
    st16_dev(ws + WS_Q + ((size_t)(2 * np + g) * 64 + tt) * 1024, l16, oq);
    if (isk) {
      const float n0 = swap_add(sq8(o0)), n1 = swap_add(sq8(o1));
      const unsigned m = umax32(f2u(hh ? n1 : n0));
      st4_dev2(ws + WS_KMAX + (head * 64 + tt) * 4, hh * 256, m);
    }
  }
  const u64 tag = [&]() LAM { u64 z = epoch + 0x9E3779B97F4A7C15ull; z = (z ^ (z >> 30)) * 0xBF58476D1CE4E5B9ull; z = (z ^ (z >> 27)) * 0x94D049BB133111EBull; return z ^ (z >> 31); }();
  TS(4);
  if (isk) WAITV(4); else WAITV(3);
  BARRIER();
  TS(5);
  u32x4 hp[4];
  const int head = xcd * 4 + (idx >> 3), qb = idx & 7;
  const int m2 = xcd * 4 + (idx & 3), n2 = idx >> 2;
  char* hb = ws + WS_H + (size_t)tt * 112 * 1024;
  const int fr0 = (blockIdx.x & 31) * 9 + tg * 3;
  const char* kv = ws + WS_KV + (size_t)head * 131072;
  f32x8 cst;
  {
    const unsigned x0 = g ? (even ? 64 : ((f2 & 1) ^ 1) * 64) : 0, x1 = g ? 0 : 64, d = g ? U : 0u - 4 * U;
    const u32x8 bases = {(wa[0] ^ x0) + d, (wa[1] ^ x0) + d, (wa[2] ^ x0) + d, (wa[3] ^ x0) + d, (wa[0] ^ x1) + d, (wa[1] ^ x1) + d, (wa[2] ^ x1) + d, (wa[3] ^ x1) + d};
    TILES(bases, g ? 3 : 2);
    if (!g) {
      put_cs(cst[0] + cst[1], 0, bias0);
      put_cs(cst[4] + cst[5], 1, bias1);
    } else {
      const char* wq = w1 + (f2 & ~1) * 64 + (size_t)(16 * tg) * 3584;
#pragma unroll 1
      for (int i = 0; i < 8; ++i) {
        glds(wq + (size_t)i * 64 * 3584, vff[0], 5 * U + i * 8192 + tg * 2048);
        glds(wq + (size_t)i * 64 * 3584, vff[1], 5 * U + i * 8192 + tg * 2048 + 1024);
      }
      TS(6);
      WAITV(16);
      cnt_add<C_DRAIN>();
      TS(7);
      if (tg == 0) {
        cnt_wait<C_DRAIN, 8>();
        st8_dev1(ws + WS_FA, (np * 16 + mt) * 8, tag);
        WAITV(1);
      } else {
        WAITV(0);
      }
    }
    TS(8);
    BARRIER();
    TS(9);
    if (!g) __builtin_amdgcn_s_setprio(2);
    TILES(bases, g ? (even ? 1 : 0) : 3);
  }
  RELANE();
  const int qg = tg & 1, kh = tg >> 1;
  u32x4 qf[4];
  unsigned kmu;
  if (g) {
    TS(10);
    __builtin_amdgcn_s_setprio(3);
    put_cs(cst[0] + cst[1], 2, bias0);
    put_cs(cst[4] + cst[5], 3, bias1);
    cnt_add<C_CS>();
    cnt_wait<C_CS, 4>();
    asm volatile("" : "={v[128:131]}"(qf[0]), "={v[132:135]}"(qf[1]), "={v[136:139]}"(qf[2]), "={v[140:143]}"(qf[3]), "={v144}"(kmu));
  } else {
  {
    const unsigned fkv = pin((head >> 2) * 16 + ch * 128 + (lane & 15)), fq = pin((head >> 1) * 16 + 2 * qb + (lane & 1));
    poll_flags<1>(ws, WS_FA + (hh ? fq : fkv) * 8, tag);
  }
  TS(10);
  {
    const char* qp = ws + WS_Q + ((size_t)head * 64 + qb * 8 + 4 * qg) * 1024;
    asm volatile("global_load_dwordx4 %0, %5, %7 sc1\n\tglobal_load_dwordx4 %1, %5, %7 offset:1024 sc1\n\tglobal_load_dwordx4 %2, %6, %7 sc1\n\t"
                 "global_load_dwordx4 %3, %6, %7 offset:1024 sc1\n\tglobal_load_dword %4, %8, %9 sc1"
                 : "=&{v[128:131]}"(qf[0]), "=&{v[132:135]}"(qf[1]), "=&{v[136:139]}"(qf[2]), "=&{v[140:143]}"(qf[3]), "=&{v144}"(kmu)
                 : "v"(l16 + kh * 2048), "v"(l16 + (1 - kh) * 2048), "s"(qp), "v"(lane * 4), "s"(ws + WS_KMAX + head * 256) : "memory");
    glds4_dev(kv + kh * 4 * U + qg * 4096, l16, kh * 4 * U + qg * 4096);
    glds4_dev(kv + kh * 4 * U + 8192 + qg * 4096, l16, kh * 4 * U + 8192 + qg * 4096);
  }
  }
  finish(accA, 2 * g, true, hp[0], hp[1]);
  finish(accB, 2 * g + 1, true, hp[2], hp[3]);
  asm volatile("" : "+v"(hp[0]), "+v"(hp[1]), "+v"(hp[2]), "+v"(hp[3]));
  TS(11);
  RELANE();
  if (g) {
    cnt_wait<C_KV1, 4>();
    TS(12);
#pragma unroll
    for (int i = 1; i < 4; ++i) { glds4_dev(kv + i * U + tg * 4096, l16, i * U + tg * 4096); glds4_dev(kv + (4 + i) * U + tg * 4096, l16, (4 + i) * U + tg * 4096); }
    if (tg < 3) {
#pragma unroll
      for (int i = 0; i < 3; ++i) {
        const int band = (fr0 + i) >> 1, nt = (blockIdx.x >> 5) * 2 + ((fr0 + i) & 1);
        glds((band < 112 ? w2 + (size_t)band * 16384 : wo + (size_t)(band - 112) * 16384) + nt * 64, (lane >> 2) * 1024 + (lane & 3) * 16, 8 * U + tg * 4096 + i * 1024);
      }
      WAITV(3);
    } else {
      WAITV(0);
    }
    cnt_add<C_KV2>();
    TS(13);
#pragma unroll
    for (int i = 0; i < 4; ++i) st16_dev(hb + (size_t)(f2 * 2 + ((even || i < 2) ? i : i - 2)) * 1024, l16, (even || i < 2) ? hp[i] : hp[i - 2]);
    if (tg < 3) {
      WAITV(4);
#pragma unroll
      for (int i = 0; i < 3; ++i) {
        const int band = (fr0 + i) >> 1, nt = (blockIdx.x >> 5) * 2 + ((fr0 + i) & 1);
        const u32x4 v = lds_trf(8 * U + tg * 4096 + i * 1024 + (8 * hh + q4) * 64 + ch * 32 + p4s * 8, 256);
        st16_dev(ws + (band < 112 ? WS_W2 + (nt * 112 + band) * 1024 : WS_WO + (nt * 32 + band - 112) * 1024), l16, v);
      }
    }
    WAITV(0);
    cnt_add<C_HDRAIN>();
    TS(14);
    if (tg == 0) {
      cnt_wait<C_HDRAIN, 8, 8>();
      st8_dev1(ws + WS_FB, blockIdx.x * 8, tag);
      TS(15);
      const unsigned fh = pin(((m2 >> 3) * 2 + ((lane >> 3) & 1)) + 8 * (((m2 >> 1) & 3) + 4 * (lane & 7))), fw = pin(32 * n2 + r);
      poll_flags<8>(ws, WS_FB + (hh ? fw : fh) * 8, tag);
      cnt_add<C_FB>();
    } else {
      cnt_wait<C_FB, 1, 8>();
    }
    TS(16);
    const auto rsrc = __builtin_amdgcn_make_buffer_rsrc(ws, 0, WS_END, 0x00020000);
    const unsigned ho[2] = {WS_H + ((m2 * 2 + (tg >> 1)) * 112 + tg * 28) * 1024, WS_H + ((m2 * 2 + 1 - (tg >> 1)) * 112 + tg * 28) * 1024};
    const unsigned wo2[2] = {WS_W2 + ((n2 * 2 + (tg & 1)) * 112 + tg * 28) * 1024, WS_W2 + ((n2 * 2 + 1 - (tg & 1)) * 112 + tg * 28) * 1024};
    constexpr int DEP = 9;
    u32x4 hf[DEP][2], wf[DEP][2];
    auto ld = [&](int s) LAM {
#pragma unroll
      for (int u = 0; u < 2; ++u) {
        hf[s % DEP][u] = __builtin_amdgcn_raw_buffer_load_b128(rsrc, l16 + (s & 3) * 1024, ho[u] + (s & ~3) * 1024, 16);
        wf[s % DEP][u] = __builtin_amdgcn_raw_buffer_load_b128(rsrc, l16 + (s & 3) * 1024, wo2[u] + (s & ~3) * 1024, 16);
      }
    };
    {
      constexpr int S0 = NBG + DEP, NL = (28 - S0) * 8;
      unsigned junk[8];
#pragma unroll
      for (int i = 0; i < 8; ++i) {
        const unsigned li = (i & 1) * 64 + lane;
        junk[i] = __builtin_amdgcn_raw_buffer_load_b32(rsrc, (li < NL ? li : 0) * 128, ((i >> 1) < 2 ? ho[(i >> 1) & 1] : wo2[(i >> 1) & 1]) + S0 * 1024, 16);
      }
#pragma unroll
      for (int i = 0; i < 8; ++i) asm volatile("" ::"v"(junk[i]));
    }
    cnt_wait<C_PROG, 4, 16>();
#pragma unroll
    for (int i = 0; i < 4; ++i)
      glds4_dev(ws + WS_WO + ((n2 * 2 + (i >> 1)) * 32 + tg * 8 + (i & 1) * 4) * 1024, l16, (i >> 1) * 4 * U + (tg * 8 + (i & 1) * 4) * 1024);
    WAITV(0);
    cnt_add<C_WO>();
    TS(17);
    f32x16 fa[2][2];
#pragma unroll
    for (int a = 0; a < 2; ++a)
#pragma unroll
      for (int b = 0; b < 2; ++b)
#pragma unroll
        for (int i = 0; i < 16; ++i) fa[a][b][i] = 0.f;
#pragma unroll
    for (int s = 0; s < DEP; ++s) ld(s);
    SB();
    __builtin_amdgcn_s_setprio(0);
#pragma unroll
    for (int s = 0; s < 28; ++s) {
      if (s == NBG) { TS(30); asm volatile("s_barrier" ::: "memory"); TS(31); }
#pragma unroll
      for (int a = 0; a < 2; ++a)
#pragma unroll
        for (int b = 0; b < 2; ++b) fa[a][b] = mfma32(wf[s % DEP][b], hf[s % DEP][a], fa[a][b]);
      SB();
      if (s + DEP < 28) ld(s + DEP);
      SB();
    }
    TS(32);
    RELANE();
    const int n0 = n2 * 64 + (tg & 1) * 32 + 8 * hh;
    const u32x4 bb0 = *(const u32x4*)(b2 + n0 * 2), bb1 = *(const u32x4*)(b2 + n0 * 2 + 32);
    auto fslot = [&](int dest, int d) LAM { const int k = dest * 3 + d - 1; return (k < 8 ? 2 * U + k * 4096 : 6 * U + (k - 8) * 4096) + l16; };
#pragma unroll
    for (int d = 1; d < 4; ++d) {
      const f32x16 v = fa[d >> 1][d & 1];
      const unsigned a = fslot(tg ^ d, d);
#pragma unroll
      for (int j = 0; j < 4; ++j) lds_wf4(a + j * 1024, (f32x4){v[4 * j], v[4 * j + 1], v[4 * j + 2], v[4 * j + 3]});
    }
    cnt_add<C_FX>();
    TS(33);
    cnt_wait<C_FX, 4>();
    TS(34);
    f32x16 z = fa[0][0];
#pragma unroll
    for (int d = 1; d < 4; ++d)
#pragma unroll
      for (int j = 0; j < 4; ++j) {
        const f32x4 v = lds_rf4(fslot(tg, d) + j * 1024);
#pragma unroll
        for (int i = 0; i < 4; ++i) z[4 * j + i] += v[i];
      }
#pragma unroll
    for (int i = 0; i < 16; ++i) {
      const unsigned bw = i < 8 ? bb0[i >> 1] : bb1[(i - 8) >> 1];
      const float y = z[i] + u2f((i & 1) ? (bw & 0xffff0000u) : (bw << 16));
      z[i] = y * __builtin_amdgcn_rcpf(1.f + __builtin_amdgcn_exp2f(y * -1.4426950408889634f));
    }
#pragma unroll
    for (int j = 0; j < 4; ++j) lds_wf4(7 * U + tg * 4096 + j * 1024 + l16, (f32x4){z[4 * j], z[4 * j + 1], z[4 * j + 2], z[4 * j + 3]});
    cnt_add<C_G>();
    TS(35);
    TSFLUSH();
    return;
  }
  RELANE();
  {
    asm volatile("s_waitcnt vmcnt(8)" : "+{v[128:131]}"(qf[0]), "+{v[132:135]}"(qf[1]), "+{v[136:139]}"(qf[2]), "+{v[140:143]}"(qf[3]), "+{v144}"(kmu)::"memory");
  }
  TS(13);
  kmu = umax32(kmu);
  { auto t2 = __builtin_amdgcn_permlane32_swap(kmu, kmu, false, false); kmu = umax(t2[0], t2[1]); }
  const unsigned qn = umax(umax(f2u(swap_add(sq8(qf[0]))), f2u(swap_add(sq8(qf[1])))),
                           umax(f2u(swap_add(sq8(qf[2]))), f2u(swap_add(sq8(qf[3])))));
  const float bound = __builtin_amdgcn_sqrtf(u2f(qn) * u2f(kmu)) * 1.001f;
  f32x16 o[4];
  float mx[4];
  unsigned ka = kh * 4 * U + lane * 16;
  const unsigned vac = pin(L_CONST + (p4 >> 1) * 8), vad = pin(kh * 4 * U + 1024 + (8 * hh + q4) * 32 + p4s * 8);
  unsigned va = ch ? vac : vad;
  const unsigned vstep = ch ? 0u : 2048u;
#pragma unroll
  for (int u = 0; u < 4; ++u)
#pragma unroll
    for (int i = 0; i < 16; ++i) o[u][i] = 0.f;
  asm volatile("" : "+v"(o[0]), "+v"(o[1]), "+v"(o[2]), "+v"(o[3]), "+v"(va));
  WAITV(0);
  cnt_add<C_KV1>();
  cnt_wait<C_KV1, 4>();
  TS(14);
  char* hst = hb + (size_t)f01 * 2048;
  if (__builtin_amdgcn_ballot_w64(bound <= BOUND_LIMIT) == ~0ull) {
    f32x16 cneg;
#pragma unroll
    for (int i = 0; i < 16; ++i) cneg[i] = -bound;
    const u32x8 q01 = __builtin_shufflevector(qf[0], qf[1], 0, 1, 2, 3, 4, 5, 6, 7), q23 = __builtin_shufflevector(qf[2], qf[3], 0, 1, 2, 3, 4, 5, 6, 7);
    asm volatile(
        "v_mov_b32 v164, 0xdddd\n\t"
        KRD("0") TR("148:149", "0") TR("150:151", "128") TR("152:153", "512") TR("154:155", "640")
        "v_mov_b32 v40, 0\n\tv_mov_b32 v41, 0\n\tv_mov_b32 v42, 0\n\tv_mov_b32 v43, 0\n\t"
        "v_mov_b32 v44, 0\n\tv_mov_b32 v45, 0\n\tv_mov_b32 v46, 0\n\tv_mov_b32 v47, 0\n\t"
        "v_mov_b32 v160, 0\n\tv_mov_b32 v161, 0\n\tv_mov_b32 v162, 0\n\tv_mov_b32 v163, 0\n\t"
        "v_mov_b32 v168, %[cnt]\n\tv_mov_b32 v169, 1\n\t"
        WT
        QK("16:31", "128:131")
        "s_mov_b32 s40, 0\n\t"
        "s_mov_b32 s41, 3\n\t"
        "s_nop 10\n\t"
        "1:\n\t"
        TILE4("160:163", "148:151", "152:155", "156:157", "158:159", "160:161", "162:163", "2048")
        TILE4("152:155", "156:159", "160:163", "148:149", "150:151", "152:153", "154:155", "4096")
        "v_add_u32 v165, 0x1000, v165\n\t"
        "s_add_u32 s40, s40, 1\n\t"
        "s_cmp_lt_u32 s40, s41\n\t"
        "s_cbranch_scc1 1b\n\t"
        "s_cmp_eq_u32 s40, 16\n\t"
        "s_cbranch_scc1 4f\n\t"
        "s_cmp_eq_u32 s40, 3\n\t"
        "s_cbranch_scc0 3f\n\t"
        "s_mov_b32 s41, 0\n\t"
        "2:\n\t"
        "ds_read_b32 v170, v168 offset:%[ckv]\n\t"
        "s_add_u32 s41, s41, 1\n\t"
        "s_waitcnt lgkmcnt(0)\n\t"
        "v_readfirstlane_b32 s42, v170\n\t"
        "s_bitcmp1_b32 s41, 22\n\t"
        "s_cselect_b32 s42, 4, s42\n\t"
        "s_cmp_lt_u32 s42, 4\n\t"
        "s_cbranch_scc1 2b\n\t"
        "global_store_dwordx4 %[l16], v[172:175], %[hst] sc1\n\t"
        "global_store_dwordx4 %[l16], v[176:179], %[hst] offset:1024 sc1\n\t"
        "global_store_dwordx4 %[l16], v[180:183], %[hst] offset:2048 sc1\n\t"
        "global_store_dwordx4 %[l16], v[184:187], %[hst] offset:3072 sc1\n\t"
        "s_mov_b32 s41, 5\n\t"
        "s_branch 1b\n\t"
        "3:\n\t"
        "s_waitcnt vmcnt(0)\n\t"
        "s_mov_b64 exec, 1\n\t"
        "ds_add_u32 v168, v169 offset:%[chd]\n\t"
        "v_add_u32 v168, 4, v168\n\t"
        "s_mov_b64 exec, -1\n\t"
        "s_cmp_eq_u32 s40, 5\n\t"
        "s_cselect_b32 s41, 8, 16\n\t"
        "s_branch 1b\n\t"
        "4:\n\t"
        PV("96:111", "160:163", "40:47")
        "s_nop 15\n\t"
        : "+{v[48:63]}"(o[0]), "+{v[64:79]}"(o[1]), "+{v[80:95]}"(o[2]), "+{v[96:111]}"(o[3]), "+{v165}"(ka), "+{v166}"(va)
        : "{v[112:127]}"(cneg), "{v[128:135]}"(q01), "{v[136:143]}"(q23), "{v167}"(vstep), [cnt] "i"(L_CNT), [chd] "i"(4 * C_HDRAIN),
          [ckv] "i"(4 * C_KV2), "{v[172:175]}"(hp[0]), "{v[176:179]}"(hp[1]), "{v[180:183]}"(hp[2]), "{v[184:187]}"(hp[3]), [l16] "v"(l16), [hst] "s"(hst)
        : "memory", "scc", "s40", "s41", "s42", "v16", "v17", "v18", "v19", "v20", "v21", "v22", "v23", "v24", "v25", "v26", "v27", "v28", "v29", "v30",
          "v31", "v32", "v33", "v34", "v35", "v36", "v37", "v38", "v39", "v40", "v41", "v42", "v43", "v44", "v45", "v46", "v47", "v144", "v145",
          "v146", "v147", "v148", "v149", "v150", "v151", "v152", "v153", "v154", "v155", "v156", "v157", "v158", "v159", "v160", "v161", "v162",
          "v163", "v164", "v168", "v169", "v170");
#pragma unroll
    for (int u = 0; u < 4; ++u) mx[u] = bound;
  } else {
#pragma unroll
    for (int i = 0; i < 4; ++i) st16_dev(hst + i * 1024, l16, hp[i]);
    WAITV(0);
    cnt_add<C_HDRAIN>();
    cnt_wait<C_KV2, 4>();
    f32x16 z;
#pragma unroll
    for (int i = 0; i < 16; ++i) z[i] = 0.f;
#pragma unroll
    for (int u = 0; u < 4; ++u) mx[u] = -3.0e38f;
#pragma unroll 1
    for (int t = 0; t < 32; ++t) {
      if (t == 16) cnt_add<C_PROG>();
      const u32x4 kf = lds_r128(ka), v0 = lds_trf(va, 128), v1 = lds_trf(va + 512, 128);
      ka += 2048; va += vstep;
#pragma unroll
      for (int u = 0; u < 4; ++u) {
        f32x16 s = mfma32(kf, qf[u], z);
        float m = s[0];
#pragma unroll
        for (int i = 1; i < 16; ++i) m = fmaxf(m, s[i]);
        m = fmaxf(swap_max(m), mx[u]);
        const float al = __builtin_amdgcn_exp2f(mx[u] - m);
        mx[u] = m;
#pragma unroll
        for (int i = 0; i < 16; ++i) { o[u][i] *= al; s[i] = __builtin_amdgcn_exp2f(s[i] - m); }
        const f32x8 plo = __builtin_shufflevector(s, s, 0, 1, 2, 3, 4, 5, 6, 7), phi = __builtin_shufflevector(s, s, 8, 9, 10, 11, 12, 13, 14, 15);
        o[u] = __builtin_amdgcn_smfmac_f32_32x32x32_bf16(__builtin_bit_cast(bf16x8, v0), __builtin_bit_cast(bf16x16, plo), o[u], 0xdddd, 0, 0);
        o[u] = __builtin_amdgcn_smfmac_f32_32x32x32_bf16(__builtin_bit_cast(bf16x8, v1), __builtin_bit_cast(bf16x16, phi), o[u], 0xdddd, 0, 0);
      }
    }
  }
  TS(30);
  RELANE();
  cnt_wait<C_WO, 4>();
#pragma unroll
  for (int u = 0; u < 2; ++u) {
    const f32x16 v = o[2 + u];
    lds_wf4(8 * U + tg * 4096 + u * 2048 + l16, (f32x4){v[0], v[1], v[2], v[3]});
    lds_wf4(8 * U + tg * 4096 + u * 2048 + 1024 + l16, (f32x4){v[4], v[5], v[6], v[7]});
    lds_wf(L_STAT + tg * 512 + u * 256 + lane * 4, hh ? mx[2 + u] : v[8]);
  }
  TS(31);
  BARRIER();
  TS(32);
  const int mo = qb * 4 + 2 * qg + kh;
#pragma unroll
  for (int u = 0; u < 2; ++u) {
    const unsigned pa = 8 * U + (tg ^ 2) * 4096 + u * 2048 + l16, za = L_STAT + (tg ^ 2) * 512 + u * 256 + r * 4;
    const f32x4 a = lds_rf4(pa), b = lds_rf4(pa + 1024);
    const float lp = lds_rf(za), mp = lds_rf(za + 128);
    const float mn = fmaxf(mx[u], mp);
    const float f0 = __builtin_amdgcn_exp2f(mx[u] - mn), f1 = __builtin_amdgcn_exp2f(mp - mn);
    const float inv = __builtin_amdgcn_rcpf(o[u][8] * f0 + lp * f1);
    const float g0 = f0 * inv, g1 = f1 * inv;
    u32x4 w;
    w[0] = pk_bf16(o[u][0] * g0 + a[0] * g1, o[u][1] * g0 + a[1] * g1);
    w[1] = pk_bf16(o[u][2] * g0 + a[2] * g1, o[u][3] * g0 + a[3] * g1);
    w[2] = pk_bf16(o[u][4] * g0 + b[0] * g1, o[u][5] * g0 + b[1] * g1);
    w[3] = pk_bf16(o[u][6] * g0 + b[2] * g1, o[u][7] * g0 + b[3] * g1);
    st16_dev(ws + WS_O + ((size_t)(mo * 2 + u) * 32 + head) * 1024, l16, w);
  }
  TS(33);
  WAITV(0);
  TS(34);
  st8_dev1(ws + WS_FC, (mo * 32 + head) * 8, tag);
  const size_t eo = ((size_t)(m2 * 64 + (tg >> 1) * 32 + r) * 512 + n2 * 64 + (tg & 1) * 32 + 8 * hh) * 2;
  const u32x4 xv0 = *(const u32x4*)(x + eo), xv1 = *(const u32x4*)(x + eo + 32);
  u32x4 wf[8][2];
#pragma unroll
  for (int s = 0; s < 8; ++s)
#pragma unroll
    for (int b = 0; b < 2; ++b) wf[s][b] = lds_r128(((b ^ (tg & 1)) * 4 * U) + (tg * 8 + s) * 1024 + l16);
  TS(35);
  poll_flags(ws, WS_FC + (m2 * 32 + tg * 8 + (lane & 7)) * 8, tag);
  TS(36);
  const auto rsrc = __builtin_amdgcn_make_buffer_rsrc(ws, 0, WS_END, 0x00020000);
  u32x4 of[8][2];
#pragma unroll
  for (int s = 0; s < 8; ++s)
#pragma unroll
    for (int a = 0; a < 2; ++a)
      of[s][a] = __builtin_amdgcn_raw_buffer_load_b128(rsrc, l16 + (s & 3) * 1024, WS_O + ((m2 * 2 + (a ^ (tg >> 1))) * 32 + tg * 8 + (s & 4)) * 1024, 16);
  SB();
  f32x16 sa[2][2];
#pragma unroll
  for (int a = 0; a < 2; ++a)
#pragma unroll
    for (int b = 0; b < 2; ++b)
#pragma unroll
      for (int i = 0; i < 16; ++i) sa[a][b][i] = 0.f;
#pragma unroll
  for (int s = 0; s < 8; ++s)
#pragma unroll
    for (int a = 0; a < 2; ++a)
#pragma unroll
      for (int b = 0; b < 2; ++b) sa[a][b] = mfma32(wf[s][b], of[s][a], sa[a][b]);
  asm volatile("" : "+v"(sa[0][0]), "+v"(sa[0][1]), "+v"(sa[1][0]), "+v"(sa[1][1]));
  TS(37);
  auto aslot = [&](int src, int d) LAM { return (d == 3 ? 4 * U : (d - 1) * 4096) + src * 8192 + l16; };
#pragma unroll
  for (int d = 1; d < 4; ++d) {
    const f32x16 v = sa[d >> 1][d & 1];
#pragma unroll
    for (int j = 0; j < 4; ++j) lds_wf4(aslot(tg, d) + j * 1024, (f32x4){v[4 * j], v[4 * j + 1], v[4 * j + 2], v[4 * j + 3]});
  }
  cnt_add<C_AX>();
  TS(38);
  cnt_wait<C_AX, 4>();
  TS(39);
  f32x16 sv = sa[0][0];
#pragma unroll
  for (int d = 1; d < 4; ++d)
#pragma unroll
    for (int j = 0; j < 4; ++j) {
      const f32x4 v = lds_rf4(aslot(tg ^ d, d) + j * 1024);
#pragma unroll
      for (int i = 0; i < 4; ++i) sv[4 * j + i] += v[i];
    }
  TS(40);
  cnt_wait<C_G, 4>();
  TS(41);
  u32x4 res[2];
#pragma unroll
  for (int j = 0; j < 4; ++j) {
    const f32x4 gv = lds_rf4(7 * U + tg * 4096 + j * 1024 + l16);
#pragma unroll
    for (int i = 0; i < 2; ++i) {
      const unsigned xw = j < 2 ? xv0[2 * j + i] : xv1[2 * (j - 2) + i];
      res[j >> 1][2 * (j & 1) + i] = pk_bf16(u2f(xw << 16) + sv[4 * j + 2 * i] * gv[2 * i],
                                             u2f(xw & 0xffff0000u) + sv[4 * j + 2 * i + 1] * gv[2 * i + 1]);
    }
  }
  *(u32x4*)(out + eo) = res[0];
  *(u32x4*)(out + eo + 32) = res[1];
  if (blockIdx.x == 0 && wid == 0) st8_dev1(ws, 0u, tag);
  TS(42);
  TSFLUSH();
  asm volatile("s_endpgm\n\t" TILE_SUB_TEXT :: "i"(L_CNT + 4 * C_DRAIN) : "memory");
}

extern "C" void kernel_launch(void* const* d_in, const int* in_sizes, int n_in, void* d_out, int out_size, void* d_ws, size_t ws_size,
                              hipStream_t stream) {
  (void)hipFuncSetAttribute((const void*)fused_kernel, hipFuncAttributeMaxDynamicSharedMemorySize, L_TOTAL);
  fused_kernel<<<256, 512, L_TOTAL, stream>>>((const char*)d_in[0], (const char*)d_in[1], (const char*)d_in[2], (const char*)d_in[4],
                                              (const char*)d_in[5], (const char*)d_in[6], (const char*)d_in[7], (char*)d_out, (char*)d_ws);
}
```
